```python
import torch

def _softmax(x, axis=-1):
    return torch.softmax(x, dim=axis)
def _gelu(x):
    return torch.nn.functional.gelu(x, approximate="tanh")
def _silu(x):
    return torch.nn.functional.silu(x)
def _sigmoid(x):
    return torch.sigmoid(x)
def _layernorm(x, axis=-1, eps=1e-5):
    m = x.mean(dim=axis, keepdim=True)
    v = x.var(dim=axis, keepdim=True, unbiased=False)
    return (x - m) / torch.sqrt(v + eps)
def _scatter_add(tgt, idx, src, axis=0):
    return tgt.clone().index_add_(axis, idx, src)
def _segment_sum(src, seg, n):
    out = torch.zeros((n,) + src.shape[1:], dtype=src.dtype)
    return out.index_add_(0, seg, src)
def _pad(x, pw):
    flat = tuple(v for pair in reversed(pw) for v in pair)
    return torch.nn.functional.pad(x, flat)

kernel_name = 'gated_mix__softmax_full_mha__dense_activation_gelu__prompt'

def setup_inputs():

    _w = (lambda a: (lambda t: t.to(torch.bfloat16) if t.is_floating_point() else t)(torch.as_tensor(a)))
    def _A_setup():
        B, S, D = 1, 2048, 512
        return {
            "x": _w((lambda *s: torch.randn(*s, dtype=torch.bfloat16) * 0.05)(B, S, D)),
            "wqkv": _w((lambda *s: torch.randn(*s, dtype=torch.bfloat16) * 0.8)(D, 3 * D)),
            "wo": _w((lambda *s: torch.randn(*s, dtype=torch.bfloat16) * 0.05)(D, D)),
        }
    _A_in = _A_setup()

    def _B_setup():
        B, S, D, F = 1, 2048, 512, 1792
        return {
            "x": _w((lambda *s: torch.randn(*s, dtype=torch.bfloat16) * 0.05)(B, S, D)),
            "w1": _w((lambda *s: torch.randn(*s, dtype=torch.bfloat16) * 3.2)(D, F) * (D ** -0.5)),
            "b1": _w((lambda *s: torch.randn(*s, dtype=torch.bfloat16) * 3.2)(F) * 0.01),
            "w2": _w((lambda *s: torch.randn(*s, dtype=torch.bfloat16) * 0.8)(F, D) * (F ** -0.5)),
            "b2": _w((lambda *s: torch.randn(*s, dtype=torch.bfloat16) * 1.6)(D) * 0.01),
        }
    _B_in = _B_setup()

    return {
        "A_x": _A_in["x"],
        "A_wqkv": _A_in["wqkv"],
        "A_wo": _A_in["wo"],
        "B_x": _B_in["x"],
        "B_w1": _B_in["w1"],
        "B_b1": _B_in["b1"],
        "B_w2": _B_in["w2"],
        "B_b2": _B_in["b2"],
        "gate_w": _w((lambda *s: torch.randn(*s, dtype=torch.bfloat16) * 0.05)(512) * 16.0),
    }

def reference(A_x, A_wqkv, A_wo, B_x, B_w1, B_b1, B_w2, B_b2, gate_w):
    def _A_ref(x, wqkv, wo):
        B, S, D = x.shape
        H = 32; Dh = D // H
        qkv = (lambda x, *d: x.permute(*d))((x @ wqkv).reshape(B, S, 3, H, Dh), 2, 0, 3, 1, 4)
        q, k, v = qkv[0], qkv[1], qkv[2]
        scale = Dh ** -0.5
        scores = torch.einsum("bhqd,bhkd->bhqk", q, k) * scale
        p = _softmax(scores, axis=-1)
        o = torch.einsum("bhqk,bhkd->bhqd", p, v)
        return (lambda x, *d: x.permute(*d))(o, 0, 2, 1, 3).reshape(B, S, D) @ wo

    def _B_ref(x, w1, b1, w2, b2):
        h = torch.einsum("bsd,df->bsf", x, w1) + b1
        h = _gelu(h)
        return torch.einsum("bsf,fd->bsd", h, w2) + b2

    x = A_x
    g = _sigmoid((x * gate_w).sum(-1, keepdims=True))
    a = _A_ref(x, A_wqkv, A_wo)
    b = _B_ref(x, B_w1, B_b1, B_w2, B_b2)
    return g * a + (1 - g) * b
```

```cpp
#include <hip/hip_runtime.h>
#include <hip/hip_bf16.h>
typedef unsigned short u16;
__device__ __forceinline__ float b2f(u16 v) { return __uint_as_float((unsigned)v << 16); }
__device__ __forceinline__ u16 f2b(float f) { unsigned u = __float_as_uint(f); u += 0x7FFFu + ((u >> 16) & 1u); return (u16)(u >> 16); }
__device__ __forceinline__ float rb(float f) { return b2f(f2b(f)); }
__global__ void ngemm(const u16* A, const u16* B, const u16* bias, u16* C, int M, int N, int K, int act) {
  int n = blockIdx.x * blockDim.x + threadIdx.x, m = blockIdx.y;
  if (n >= N) return;
  float acc = 0;
  for (int k = 0; k < K; k++) acc += b2f(A[m * K + k]) * b2f(B[k * N + n]);
  acc = rb(acc);
  if (bias) acc = rb(acc + b2f(bias[n]));
  if (act) { float x = acc; acc = 0.5f * x * (1.f + tanhf(0.7978845608028654f * (x + 0.044715f * x * x * x))); }
  C[m * N + n] = f2b(acc);
}
__global__ void nattn(const u16* qkv, u16* o) {
  int q = blockIdx.x, h = blockIdx.y, t = threadIdx.x;
  __shared__ float s[2048]; __shared__ float red[256];
  float qv[16];
  for (int d = 0; d < 16; d++) qv[d] = b2f(qkv[q * 1536 + h * 16 + d]);
  float mx = -1e30f;
  for (int k = t; k < 2048; k += 256) { float a = 0; for (int d = 0; d < 16; d++) a += qv[d] * b2f(qkv[k * 1536 + 512 + h * 16 + d]); a = rb(rb(a) * 0.25f); s[k] = a; mx = fmaxf(mx, a); }
  red[t] = mx; __syncthreads();
  for (int o2 = 128; o2; o2 >>= 1) { if (t < o2) red[t] = fmaxf(red[t], red[t + o2]); __syncthreads(); }
  mx = red[0]; __syncthreads();
  float sm = 0;
  for (int k = t; k < 2048; k += 256) { float e = expf(s[k] - mx); s[k] = e; sm += e; }
  red[t] = sm; __syncthreads();
  for (int o2 = 128; o2; o2 >>= 1) { if (t < o2) red[t] += red[t + o2]; __syncthreads(); }
  sm = red[0]; __syncthreads();
  int d = t & 15, part = t >> 4;
  float a = 0;
  for (int k = part; k < 2048; k += 16) a += rb(s[k] / sm) * b2f(qkv[k * 1536 + 1024 + h * 16 + d]);
  red[t] = a; __syncthreads();
  if (t < 16) { float r = 0; for (int i = 0; i < 16; i++) r += red[i * 16 + t]; o[q * 512 + h * 16 + t] = f2b(r); }
}
__global__ void ncomb(const u16* x, const u16* gw, const u16* a, const u16* b, u16* out) {
  int r = blockIdx.x, t = threadIdx.x;
  __shared__ float red[512];
  red[t] = rb(b2f(x[r * 512 + t]) * b2f(gw[t])); __syncthreads();
  for (int o2 = 256; o2; o2 >>= 1) { if (t < o2) red[t] += red[t + o2]; __syncthreads(); }
  float z = rb(red[0]); float g = rb(1.f / (1.f + expf(-z)));
  float og = rb(1.f - g);
  out[r * 512 + t] = f2b(rb(g * b2f(a[r * 512 + t])) + rb(og * b2f(b[r * 512 + t])));
}
static void naive_launch(void* const* d_in, void* d_out, void* d_ws, hipStream_t stream) {
  const u16 *x = (const u16*)d_in[0], *wqkv = (const u16*)d_in[1], *wo = (const u16*)d_in[2], *w1 = (const u16*)d_in[4], *b1 = (const u16*)d_in[5], *w2 = (const u16*)d_in[6], *b2 = (const u16*)d_in[7], *gw = (const u16*)d_in[8];
  u16* ws = (u16*)d_ws;
  u16 *qkv = ws, *o = qkv + 2048 * 1536, *a = o + 2048 * 512, *h = a + 2048 * 512, *b = h + 2048 * 1792;
  ngemm<<<dim3(6, 2048), 256, 0, stream>>>(x, wqkv, nullptr, qkv, 2048, 1536, 512, 0);
  nattn<<<dim3(2048, 32), 256, 0, stream>>>(qkv, o);
  ngemm<<<dim3(2, 2048), 256, 0, stream>>>(o, wo, nullptr, a, 2048, 512, 512, 0);
  ngemm<<<dim3(7, 2048), 256, 0, stream>>>(x, w1, b1, h, 2048, 1792, 512, 1);
  ngemm<<<dim3(2, 2048), 256, 0, stream>>>(h, w2, b2, b, 2048, 512, 1792, 0);
  ncomb<<<2048, 512, 0, stream>>>(x, gw, a, b, (u16*)d_out);
}
extern "C" void kernel_launch(void* const* d_in, const int* in_sizes, int n_in, void* d_out, int out_size, void* d_ws, size_t ws_size, hipStream_t stream) {
  naive_launch(d_in, d_out, d_ws, stream);
}
```

```cpp
#include <hip/hip_runtime.h>
#include <hip/hip_bf16.h>
#include <stdint.h>

typedef unsigned short u16;
typedef unsigned int u32;
typedef unsigned long long u64;
typedef __attribute__((ext_vector_type(8))) __bf16 bf16x8;
typedef __attribute__((ext_vector_type(4))) short s16x4;
typedef __attribute__((ext_vector_type(4))) float f32x4;
typedef __attribute__((ext_vector_type(2))) float f32x2;
typedef __attribute__((ext_vector_type(16))) float f32x16;
typedef __attribute__((ext_vector_type(4))) u32 u32x4;
typedef __attribute__((ext_vector_type(2))) u32 u32x2;
#define LAS __attribute__((address_space(3)))
#define GAS __attribute__((address_space(1)))
typedef LAS char* lds_ptr;

#define MODE 0

constexpr int SEQ = 2048, DM = 512, FF = 1792;
constexpr u32 WS_EPOCH = 0, WS_FLAG1 = 4096, WS_FLAG2 = 8192, WS_G = 16384, WS_STAMP = 32768;
constexpr u32 WS_Q = 1u << 20, WS_KV = 3u << 20, WS_H = 7u << 20, WS_W2F = 14u << 20, WS_WOF = 16u << 20, WS_O = 17u << 20, WS_END = 19u << 20;
constexpr int L_WQ = 0, L_W1A = 96 * 1024, L_W1B = 40 * 1024, L_SP = 152 * 1024, L_X2 = 104 * 1024, KVS = 2176, LDS_BYTES = 160 * 1024;
constexpr int AUX_SC1 = 16;
constexpr u32 SPIN_CAP = 1u << 15;
constexpr float QSCALE = 0.25f * 1.4426950408889634f;

#define SBAR() __builtin_amdgcn_sched_barrier(0)
#define PIN(x) asm volatile("" : "+v"(x))
#define KEEP(x) asm volatile("" :: "v"(x))
#define MFMA32(a, b, c) __builtin_amdgcn_mfma_f32_32x32x16_bf16(a, b, c, 0, 0, 0)
#define MFMA16(a, b, c) __builtin_amdgcn_mfma_f32_16x16x32_bf16(a, b, c, 0, 0, 0)
#define BARRIER() asm volatile("s_waitcnt lgkmcnt(0)\n\ts_barrier" ::: "memory")
#define RLX_AGENT __ATOMIC_RELAXED, __HIP_MEMORY_SCOPE_AGENT

#define STAMP(i) do { } while (0)

__device__ __forceinline__ float bf2f(u32 v) { return __uint_as_float(v << 16); }
__device__ __forceinline__ u32 cvtpk(float lo, float hi) {
  typedef __attribute__((ext_vector_type(2))) __bf16 bf16x2;
  bf16x2 r = {(__bf16)lo, (__bf16)hi};
  return __builtin_bit_cast(u32, r);
}
__device__ __forceinline__ float rbf(float f) { return bf2f(cvtpk(f, f) & 0xffffu); }
__device__ __forceinline__ bf16x8 asbf(u32x4 v) { return __builtin_bit_cast(bf16x8, v); }
__device__ __forceinline__ void glds16(const void* sbase, u32 voff, u32 lds_base) {
  u32 sv;
  asm volatile("s_mov_b32 %0, m0\n\ts_mov_b32 m0, %3\n\ts_nop 0\n\tglobal_load_lds_dwordx4 %1, %2\n\ts_mov_b32 m0, %0"
               : "=&s"(sv) : "v"(voff), "s"(sbase), "s"(lds_base) : "memory");
}
__device__ __forceinline__ bf16x8 trfrag(lds_ptr p0, lds_ptr p1) {
  typedef short v4i16_t __attribute__((ext_vector_type(4)));
  v4i16_t a = __builtin_amdgcn_ds_read_tr16_b64_v4i16((LAS v4i16_t*)p0);
  v4i16_t b = __builtin_amdgcn_ds_read_tr16_b64_v4i16((LAS v4i16_t*)p1);
  typedef short v8i16_t __attribute__((ext_vector_type(8)));
  v8i16_t r = __builtin_shufflevector(a, b, 0, 1, 2, 3, 4, 5, 6, 7);
  return __builtin_bit_cast(bf16x8, r);
}
__device__ __forceinline__ u32x4 lds_ld16(lds_ptr p) { return *(LAS u32x4*)p; }
__device__ __forceinline__ void lds_st16(lds_ptr p, u32x4 v) { *(LAS u32x4*)p = v; }
__device__ __forceinline__ u32x4 pair16(f32x4 a, f32x4 b) {
  u32 a0 = cvtpk(a[0], a[1]), a1 = cvtpk(a[2], a[3]), b0 = cvtpk(b[0], b[1]), b1 = cvtpk(b[2], b[3]);
  auto s0 = __builtin_amdgcn_permlane16_swap(a0, b0, false, false);
  auto s1 = __builtin_amdgcn_permlane16_swap(a1, b1, false, false);
  return u32x4{s0[0], s1[0], s0[1], s1[1]};
}
__device__ __forceinline__ u32x4 pair32(f32x4 a, f32x4 b) {
  u32 a0 = cvtpk(a[0], a[1]), a1 = cvtpk(a[2], a[3]), b0 = cvtpk(b[0], b[1]), b1 = cvtpk(b[2], b[3]);
  auto s0 = __builtin_amdgcn_permlane32_swap(a0, b0, false, false);
  auto s1 = __builtin_amdgcn_permlane32_swap(a1, b1, false, false);
  return u32x4{s0[0], s1[0], s0[1], s1[1]};
}
__device__ __forceinline__ float gelu1(float x) {
  constexpr float K0 = -2.0f * 0.7978845608028654f * 1.4426950408889634f, K1 = K0 * 0.044715f;
  float z = x * __builtin_fmaf(x * x, K1, K0);
  return x * __builtin_amdgcn_rcpf(1.0f + __builtin_amdgcn_exp2f(z));
}
__device__ __forceinline__ float xhalf_max(float m) {
  auto r = __builtin_amdgcn_permlane32_swap(__float_as_uint(m), __float_as_uint(m), false, false);
  return __builtin_fmaxf(__uint_as_float(r[0]), __uint_as_float(r[1]));
}
__device__ __forceinline__ float max16(const f32x16& s) {
  float a = __builtin_fmaxf(s[0], s[1]), b = __builtin_fmaxf(s[2], s[3]);
#pragma unroll
  for (int r = 4; r < 16; r += 4) { a = __builtin_fmaxf(__builtin_fmaxf(a, s[r]), s[r + 1]); b = __builtin_fmaxf(__builtin_fmaxf(b, s[r + 2]), s[r + 3]); }
  return __builtin_fmaxf(a, b);
}
__device__ __forceinline__ f32x16 splat16(float v) { return f32x16{v, v, v, v, v, v, v, v, v, v, v, v, v, v, v, v}; }

template <int M>
__global__ void __launch_bounds__(512, 2)
fused_kernel(const u16* __restrict__ x, const u16* __restrict__ wqkv, const u16* __restrict__ wo, const u16* __restrict__ w1,
             const u16* __restrict__ b1, const u16* __restrict__ w2, const u16* __restrict__ b2, const u16* __restrict__ gw,
             u16* __restrict__ out, char* ws) {
  __shared__ __attribute__((aligned(16))) char smem[LDS_BYTES];
  const lds_ptr lds = (lds_ptr)smem;
  const u32 lds0 = (u32)(uintptr_t)lds;
  const int tid = threadIdx.x, lane = tid & 63, w = __builtin_amdgcn_readfirstlane(tid >> 6);
  const int b = blockIdx.x, xc = b & 7, bi = b >> 3;
  const int r16 = lane & 15, g4 = lane >> 4, r32 = lane & 31, hh = lane >> 5;
  const __amdgpu_buffer_rsrc_t rs = __builtin_amdgcn_make_buffer_rsrc(ws, 0, WS_END, 0x00020000);
  const int head = xc * 4 + (bi >> 3), qb = bi & 7;
  const int tb = (xc & 3) * 8 + (bi & 7), nb = (xc >> 2) * 4 + (bi >> 3);
  bool alive = true;
  u64 tag = 0;
  STAMP(0);

  if constexpr (M != 2) {
    const int rb = (xc & 1) * 8 + (bi & 7), cb = (xc >> 1) * 4 + (bi >> 3);
    const int rg = w & 3, kh = w >> 2;
    u32x4 xf[2][8];
    const u32 xo0 = (u32)(((rb * 128 + rg * 32 + 16 * kh + r16) * DM + kh * 256 + g4 * 8) * 2);
    const u32 xo1 = (u32)(((rb * 128 + rg * 32 + 16 * (kh ^ 1) + r16) * DM + kh * 256 + g4 * 8) * 2);
    const u32 wql = (u32)(((lane >> 2) * 1536 + ((lane & 3) ^ (hh << 1)) * 8) * 2);
#define XLD(rt, ks) asm volatile("global_load_dwordx4 %0, %1, %2 offset:%3" : "=v"(xf[rt][ks]) : "v"(rt ? xo1 : xo0), "s"(x), "i"((ks) * 64))
#define STAGE1(s) do { XLD(0, 2 * s); XLD(1, 2 * s); XLD(0, 2 * s + 1); XLD(1, 2 * s + 1); \
    _Pragma("unroll") for (int j = 0; j < 3; ++j) { const int id = w * 3 + j, blk = id >> 2, T = blk >> 1, k0 = (blk & 1) * 256 + (id & 3) * 16 + (s) * 64; \
      glds16((const char*)wqkv + (k0 * 1536 + T * 512 + cb * 32) * 2, wql, lds0 + L_WQ + T * 32768 + k0 * 64); } } while (0)
    STAGE1(0); STAGE1(1); STAGE1(2); STAGE1(3);
    u32 w1o[7];
#pragma unroll
    for (int j = 0; j < 7; ++j) {
      const int c = (w * 7 + j) * 64 + lane, pos = c / 14, cc = c - pos * 14;
      const int lr = (pos & ~12) | ((pos & 4) << 1) | ((pos & 8) >> 1);
      w1o[j] = (u32)((((lr >> 7) * 256 + (lr & 127)) * FF + cc * 8) * 2);
      glds16((const char*)w1 + cb * 224, w1o[j], lds0 + L_W1A + (w * 7 + j) * 1024);
    }
    const u64 epoch = __hip_atomic_load((GAS u64*)(ws + WS_EPOCH), RLX_AGENT);
    const int grow = rb * 128 + cb * 8 + w;
    const u32x4 gx = *(const u32x4*)(x + grow * DM + lane * 8), gv = *(const u32x4*)(gw + lane * 8);
    u32x2 b1v[7];
#pragma unroll
    for (int c = 0; c < 7; ++c) b1v[c] = *(const u32x2*)(b1 + cb * 112 + c * 16 + g4 * 4);
    u32 pt[10];
    {
      const int f = b * 9 + w, fo = f - 1792;
      const bool isw2 = f < 1792;
      const u16* src = isw2 ? w2 + ((f % 112) * 16 + 8 * hh) * DM + (f / 112) * 32 + r32 : wo + ((fo & 31) * 16 + 4 * hh) * DM + (fo >> 5) * 32 + r32;
      const int hi = isw2 ? 4 * DM : 8 * DM;
#pragma unroll
      for (int j = 0; j < 4; ++j) { pt[j] = src[j * DM]; pt[4 + j] = src[hi + j * DM]; }
    }
    {
      const int f = b * 9 + 8, fo = f - 1792;
      const bool isw2 = f < 1792;
      const u16* src = isw2 ? w2 + ((f % 112) * 16 + 8 * hh + 2 * (w & 3)) * DM + (f / 112) * 32 + r32
                            : wo + ((fo & 31) * 16 + 4 * hh + 2 * (w & 1) + 4 * (w & 2)) * DM + (fo >> 5) * 32 + r32;
      pt[8] = src[0]; pt[9] = src[DM];
    }

    f32x4 aq[3][2][2];
#pragma unroll
    for (int T = 0; T < 3; ++T)
#pragma unroll
      for (int h = 0; h < 2; ++h) { aq[T][h][0] = f32x4{}; aq[T][h][1] = f32x4{}; }
    const lds_ptr wqb = lds + L_WQ + (kh * 256 + 8 * g4 + (r16 >> 2)) * 64 + (r16 & 1) * 8;
    const lds_ptr wqb0 = wqb + ((((r16 >> 1) & 1)) ^ ((g4 & 1) << 1)) * 16, wqb1 = wqb + ((2 + ((r16 >> 1) & 1)) ^ ((g4 & 1) << 1)) * 16;
#define WAITX(n, s) asm volatile("s_waitcnt vmcnt(" #n ")\n\ts_barrier" : "+v"(xf[0][2 * s]), "+v"(xf[1][2 * s]), "+v"(xf[0][2 * s + 1]), "+v"(xf[1][2 * s + 1]) :: "memory")
#define QKVSTEP(ks) do { _Pragma("unroll") for (int T = 0; T < 3; ++T) _Pragma("unroll") for (int h = 0; h < 2; ++h) { \
      const lds_ptr p = (h ? wqb1 : wqb0) + T * 32768 + (ks) * 2048; const bf16x8 wf = trfrag(p, p + 256); \
      _Pragma("unroll") for (int rt = 0; rt < 2; ++rt) aq[T][h][rt] = (T < 2) ? MFMA16(wf, asbf(xf[rt][ks]), aq[T][h][rt]) : MFMA16(asbf(xf[rt][ks]), wf, aq[T][h][rt]); } } while (0)
    STAMP(13);
    WAITX(48, 0); STAMP(1); QKVSTEP(0); QKVSTEP(1);
    WAITX(41, 1); QKVSTEP(2); QKVSTEP(3);
    WAITX(34, 2); QKVSTEP(4); QKVSTEP(5);
    WAITX(27, 3); STAMP(2); QKVSTEP(6); QKVSTEP(7);
    BARRIER();
#define X1ADDR(ww, j) (lds + (((ww) * 6 + (j)) < 40 ? ((ww) * 6 + (j)) * 1024 : L_SP + ((ww) * 6 + (j) - 40) * 1024) + lane * 16)
#pragma unroll
    for (int j = 0; j < 6; ++j) lds_st16(X1ADDR(w, j), __builtin_bit_cast(u32x4, aq[j >> 1][j & 1][1]));
    {
      float s = 0.f;
#pragma unroll
      for (int e = 0; e < 4; ++e) { s += rbf(bf2f(gx[e] & 0xffffu) * bf2f(gv[e] & 0xffffu)); s += rbf(bf2f(gx[e] >> 16) * bf2f(gv[e] >> 16)); }
#pragma unroll
      for (int o = 32; o; o >>= 1) s += __shfl_xor(s, o);
      const float z = rbf(s), g = rbf(1.0f / (1.0f + __expf(-z)));
      if (lane == 0) __builtin_amdgcn_raw_buffer_store_b32(__float_as_uint(g), rs, WS_G + grow * 4, 0, AUX_SC1);
    }
    __builtin_amdgcn_raw_buffer_store_b128(u32x4{pt[0] | (pt[1] << 16), pt[2] | (pt[3] << 16), pt[4] | (pt[5] << 16), pt[6] | (pt[7] << 16)}, rs,
                                           WS_W2F + (b * 9 + w) * 1024 + ((b * 9 + w) < 1792 ? 0 : WS_WOF - WS_W2F - 1792 * 1024) + lane * 16, 0, AUX_SC1);
    if (w < 4) __builtin_amdgcn_raw_buffer_store_b32(pt[8] | (pt[9] << 16), rs, WS_W2F + (b * 9 + 8) * 1024 + ((b * 9 + 8) < 1792 ? 0 : WS_WOF - WS_W2F - 1792 * 1024) + lane * 16 + w * 4, 0, AUX_SC1);
    __builtin_amdgcn_raw_buffer_store_b128(u32x4{~0u, ~0u, ~0u, ~0u}, rs, WS_O + ((qb * 8 + w) * 32 + head) * 1024 + lane * 16, 0, AUX_SC1);
    tag = (((u64)__builtin_amdgcn_readfirstlane((u32)(epoch >> 32)) << 32) | (u64)__builtin_amdgcn_readfirstlane((u32)epoch)) + 1;
    BARRIER();
    {
      f32x4 v[6];
#pragma unroll
      for (int j = 0; j < 6; ++j) v[j] = aq[j >> 1][j & 1][0] + __builtin_bit_cast(f32x4, lds_ld16(X1ADDR(w ^ 4, j)));
      const int tile = rb * 4 + rg;
      const u32 qk = (u32)(((g4 >> 1) * 32 + 16 * kh + r16) * 16);
      __builtin_amdgcn_raw_buffer_store_b128(pair16(v[2], v[3]), rs, WS_KV + (cb * 2 + (g4 & 1)) * 131072 + tile * 2048 + qk, 0, AUX_SC1);
      __builtin_amdgcn_raw_buffer_store_b128(pair32(v[4], v[5]), rs, WS_KV + (cb * 2 + hh) * 131072 + tile * 2048 + 1024 + (kh * 32 + r32) * 16, 0, AUX_SC1);
      __builtin_amdgcn_raw_buffer_store_b128(pair16(v[0] * QSCALE, v[1] * QSCALE), rs, WS_Q + ((cb * 2 + (g4 & 1)) * 64 + tile) * 1024 + qk, 0, AUX_SC1);
    }
    STAMP(3);
#pragma unroll
    for (int j = 0; j < 7; ++j) glds16((const char*)w1 + cb * 224 + 128 * FF * 2, w1o[j], lds0 + L_W1B + (w * 7 + j) * 1024);

    f32x4 ah[7][2];
#pragma unroll
    for (int c = 0; c < 7; ++c) {
      ah[c][0] = f32x4{bf2f(b1v[c][0] & 0xffffu), bf2f(b1v[c][0] >> 16), bf2f(b1v[c][1] & 0xffffu), bf2f(b1v[c][1] >> 16)};
      ah[c][1] = f32x4{};
    }
    const int w1l = (kh * 128 + 16 * (g4 >> 1) + 4 * (g4 & 1) + (r16 >> 2)) * 224 + (r16 & 3) * 8;
#define FFN1STEP(base, ks) do { _Pragma("unroll") for (int c = 0; c < 7; ++c) { \
      const lds_ptr p = lds + (base) + w1l + ((ks) & 3) * (32 * 224) + c * 32; const bf16x8 wf = trfrag(p, p + 8 * 224); \
      ah[c][0] = MFMA16(wf, asbf(xf[0][ks]), ah[c][0]); ah[c][1] = MFMA16(wf, asbf(xf[1][ks]), ah[c][1]); } } while (0)
    FFN1STEP(L_W1A, 0); FFN1STEP(L_W1A, 1);
    asm volatile("s_waitcnt vmcnt(7)\n\ts_barrier" ::: "memory");
    if (tid == 0) __hip_atomic_store((GAS u64*)(ws + WS_FLAG1 + b * 8), tag, RLX_AGENT);
    STAMP(4);
    FFN1STEP(L_W1A, 2); FFN1STEP(L_W1A, 3);
    asm volatile("s_waitcnt vmcnt(0)\n\ts_barrier" ::: "memory");
    FFN1STEP(L_W1B, 4); FFN1STEP(L_W1B, 5); FFN1STEP(L_W1B, 6); FFN1STEP(L_W1B, 7);
    STAMP(5);
    BARRIER();
#pragma unroll
    for (int c = 0; c < 7; ++c) lds_st16(lds + L_X2 + (w * 7 + c) * 1024 + lane * 16, __builtin_bit_cast(u32x4, ah[c][1]));
    BARRIER();
#pragma unroll
    for (int c = 0; c < 7; ++c) ah[c][0] += __builtin_bit_cast(f32x4, lds_ld16(lds + L_X2 + ((w ^ 4) * 7 + c) * 1024 + lane * 16));
    u32x4 kv[16];
    if constexpr (M == 0) {
      for (u32 spins = 0;;) {
        const u32x4 f0 = __builtin_amdgcn_raw_buffer_load_b128(rs, WS_FLAG1 + lane * 32, 0, AUX_SC1);
        const u32x4 f1 = __builtin_amdgcn_raw_buffer_load_b128(rs, WS_FLAG1 + lane * 32 + 16, 0, AUX_SC1);
        const u32 lo = (u32)tag, hi = (u32)(tag >> 32);
        const bool ok = f0[0] == lo && f0[1] == hi && f0[2] == lo && f0[3] == hi && f1[0] == lo && f1[1] == hi && f1[2] == lo && f1[3] == hi;
        if (__all(ok)) break;
        if (++spins > SPIN_CAP) { alive = false; break; }
        __builtin_amdgcn_s_sleep(2);
      }
      asm volatile("" ::: "memory");
      STAMP(6);
      if (b == 0 && tid == 0 && alive) __hip_atomic_store((GAS u64*)(ws + WS_EPOCH), tag, RLX_AGENT);
#pragma unroll
      for (int j = 0; j < 16; ++j) kv[j] = __builtin_amdgcn_raw_buffer_load_b128(rs, WS_KV + head * 131072 + (j * 8 + w) * 1024 + lane * 16, 0, AUX_SC1);
    }
    BARRIER();
    {
#pragma unroll
      for (int c = 0; c < 7; ++c)
#pragma unroll
        for (int r = 0; r < 4; ++r) ah[c][0][r] = gelu1(ah[c][0][r]);
      const u32 hb = WS_H + (u32)(((rb * 4 + rg) * 112 + cb * 7) * 1024 + ((g4 >> 1) * 32 + 16 * kh + r16) * 16);
#pragma unroll
      for (int c = 0; c < 6; c += 2) __builtin_amdgcn_raw_buffer_store_b128(pair16(ah[c][0], ah[c + 1][0]), rs, hb + (c + (g4 & 1)) * 1024, 0, AUX_SC1);
      __builtin_amdgcn_raw_buffer_store_b64(u32x2{cvtpk(ah[6][0][0], ah[6][0][1]), cvtpk(ah[6][0][2], ah[6][0][3])}, rs, hb + 6 * 1024 + (g4 & 1) * 8, 0, AUX_SC1);
    }
    STAMP(7);
    if constexpr (M == 0) {
#pragma unroll
      for (int j = 0; j < 16; ++j) lds_st16(lds + ((j * 8 + w) >> 1) * KVS + (w & 1) * 1024 + lane * 16, kv[j]);
    }
  }
  if constexpr (M == 2) {
#pragma unroll
    for (int j = 0; j < 16; ++j)
      lds_st16(lds + ((j * 8 + w) >> 1) * KVS + (w & 1) * 1024 + lane * 16,
               __builtin_amdgcn_raw_buffer_load_b128(rs, WS_KV + head * 131072 + (j * 8 + w) * 1024 + lane * 16, 0, AUX_SC1));
  }
  if constexpr (M != 1) {
    if ((tid & 7) < 6) { const u32 one = ((tid & 3) == 0) ? 0x3F803F80u : 0u; lds_st16(lds + (tid >> 3) * KVS + 2048 + (tid & 7) * 16, u32x4{one, one, one, one}); }
    const bf16x8 qf = asbf(__builtin_amdgcn_raw_buffer_load_b128(rs, WS_Q + (head * 64 + qb * 8 + w) * 1024 + lane * 16, 0, AUX_SC1));
    const float gq = __uint_as_float(__builtin_amdgcn_raw_buffer_load_b32(rs, WS_G + (qb * 256 + w * 32 + r32) * 4, 0, AUX_SC1));
    const int tb2 = w & 1, kq = w >> 1;
    const float gt = __uint_as_float(__builtin_amdgcn_raw_buffer_load_b32(rs, WS_G + (tb * 64 + tb2 * 32 + r32) * 4, 0, AUX_SC1));
    asm volatile("s_waitcnt vmcnt(0) lgkmcnt(0)\n\ts_barrier" ::: "memory");
    if constexpr (M == 0) { if (tid == 0) __hip_atomic_store((GAS u64*)(ws + WS_FLAG2 + b * 8), tag, RLX_AGENT); }
    STAMP(8);

    lds_ptr kp = lds + lane * 16;
    const bool grp2 = (r32 >= 16 && r32 < 20) || r32 >= 28;
    const int coff = 2048 + (r32 == 16 ? 0 : r32 == 20 ? 64 : grp2 ? 16 : 80);
    lds_ptr vp0 = lds + (r32 < 16 ? 1024 + hh * 256 + r32 * 16 : coff), vp1 = lds + (r32 < 16 ? 1536 + hh * 256 + r32 * 16 : coff);
    f32x16 sA, sB, oacc = splat16(0.f), cneg;
    f32x16 fa0 = splat16(0.f), fa1 = splat16(0.f);
    u32x4 kA, kB, vA0, vA1, vB0, vB1;
    u32x4 sl[4][3];
    {
      kA = lds_ld16(kp);
      sB = MFMA32(asbf(kA), qf, splat16(0.f));
      cneg = splat16(-xhalf_max(max16(sB)));
      PIN(cneg);
      sA = MFMA32(asbf(kA), qf, cneg);
      kA = lds_ld16(kp + KVS); vA0 = lds_ld16(vp0); vA1 = lds_ld16(vp1);
    }
    const u32 hoff = WS_H + (u32)(((tb * 2 + tb2) * 112 + kq * 28) * 1024) + lane * 16;
    const u32 woff = WS_W2F + (u32)((nb * 2 * 112 + kq * 28) * 1024) + lane * 16;
#define EX(v) __builtin_amdgcn_exp2f(v)
#define TILE(SC, SN, KC, KN, VC0, VC1, VN0, VN1, u, EXTRA) do { \
      SN = MFMA32(asbf(KC), qf, cneg); KEEP(SN); \
      KN = lds_ld16(kp + ((u) + 2) * KVS); VN0 = lds_ld16(vp0 + ((u) + 1) * KVS); VN1 = lds_ld16(vp1 + ((u) + 1) * KVS); \
      SBAR(); \
      u32x4 p0, p1; \
      _Pragma("unroll") for (int r = 0; r < 8; ++r) SC[r] = EX(SC[r]); \
      p0 = u32x4{cvtpk(SC[0], SC[1]), cvtpk(SC[2], SC[3]), cvtpk(SC[4], SC[5]), cvtpk(SC[6], SC[7])}; \
      SC[8] = EX(SC[8]); SC[9] = EX(SC[9]); PIN(p0); PIN(SC); \
      SBAR(); \
      oacc = MFMA32(asbf(VC0), asbf(p0), oacc); KEEP(oacc); \
      SBAR(); \
      _Pragma("unroll") for (int r = 10; r < 16; ++r) SC[r] = EX(SC[r]); \
      p1 = u32x4{cvtpk(SC[8], SC[9]), cvtpk(SC[10], SC[11]), cvtpk(SC[12], SC[13]), cvtpk(SC[14], SC[15])}; PIN(p1); \
      SBAR(); \
      EXTRA; \
      oacc = MFMA32(asbf(VC1), asbf(p1), oacc); KEEP(oacc); \
      SBAR(); } while (0)
#define TA(u, EXTRA) TILE(sA, sB, kA, kB, vA0, vA1, vB0, vB1, u, EXTRA)
#define TB(u, EXTRA) TILE(sB, sA, kB, kA, vB0, vB1, vA0, vA1, u, EXTRA)
#define ADV() do { kp += 8 * KVS; vp0 += 8 * KVS; vp1 += 8 * KVS; } while (0)
#define SLOAD(s, so) do { sl[s][0] = __builtin_amdgcn_raw_buffer_load_b128(rs, hoff, (so) + (s) * 1024, AUX_SC1); \
      sl[s][1] = __builtin_amdgcn_raw_buffer_load_b128(rs, woff, (so) + (s) * 1024, AUX_SC1); \
      sl[s][2] = __builtin_amdgcn_raw_buffer_load_b128(rs, woff, (so) + (s) * 1024 + 112 * 1024, AUX_SC1); } while (0)
#define F0(s) fa0 = MFMA32(asbf(sl[s][1]), asbf(sl[s][0]), fa0); KEEP(fa0)
#define F1(s) fa1 = MFMA32(asbf(sl[s][2]), asbf(sl[s][0]), fa1); KEEP(fa1)
    TA(0, ); TB(1, ); TA(2, ); TB(3, ); TA(4, ); TB(5, ); TA(6, ); TB(7, ); ADV();
    if constexpr (M == 0) {
      const int prb = tb >> 1, pcb = r16;
      const int pb = ((pcb >> 2) << 1 | (prb >> 3)) | (((pcb & 3) << 3 | (prb & 7)) << 3);
      for (u32 spins = 0; alive;) {
        const u64 f = __hip_atomic_load((GAS u64*)(ws + WS_FLAG2 + pb * 8), RLX_AGENT);
        if (__all(f == tag)) break;
        if (++spins > SPIN_CAP) { alive = false; break; }
        __builtin_amdgcn_s_sleep(2);
      }
      asm volatile("" ::: "memory");
    }
    STAMP(9);
    SLOAD(0, 0); SLOAD(1, 0); SLOAD(2, 0); SLOAD(3, 0);
    for (int it = 1; it < 7; ++it) {
      const int so = it * 4096;
      TA(0, F0(0)); TB(1, F1(0); SLOAD(0, so)); TA(2, F0(1)); TB(3, F1(1); SLOAD(1, so));
      TA(4, F0(2)); TB(5, F1(2); SLOAD(2, so)); TA(6, F0(3)); TB(7, F1(3); SLOAD(3, so)); ADV();
    }
    TA(0, F0(0)); TB(1, F1(0)); TA(2, F0(1)); TB(3, F1(1)); TA(4, F0(2)); TB(5, F1(2)); TA(6, F0(3)); TB(7, F1(3));
    STAMP(10);
    if (__builtin_expect(!__all(oacc[8] <= 1073741824.0f), 0)) {
      lds_ptr k2 = lds + lane * 16, v2 = vp0 - 56 * KVS, v3 = vp1 - 56 * KVS;
      float m = -INFINITY;
      for (int t = 0; t < 64; ++t) m = __builtin_fmaxf(m, max16(MFMA32(asbf(lds_ld16(k2 + t * KVS)), qf, splat16(0.f))));
      cneg = splat16(-xhalf_max(m));
      oacc = splat16(0.f);
      for (int t = 0; t < 64; ++t) {
        f32x16 s = MFMA32(asbf(lds_ld16(k2 + t * KVS)), qf, cneg);
#pragma unroll
        for (int r = 0; r < 16; ++r) s[r] = EX(s[r]);
        oacc = MFMA32(asbf(lds_ld16(v2 + t * KVS)), asbf(u32x4{cvtpk(s[0], s[1]), cvtpk(s[2], s[3]), cvtpk(s[4], s[5]), cvtpk(s[6], s[7])}), oacc);
        oacc = MFMA32(asbf(lds_ld16(v3 + t * KVS)), asbf(u32x4{cvtpk(s[8], s[9]), cvtpk(s[10], s[11]), cvtpk(s[12], s[13]), cvtpk(s[14], s[15])}), oacc);
      }
    }
    {
      const float sc = gq / oacc[8];
      u32x4 o = u32x4{cvtpk(oacc[0] * sc, oacc[1] * sc), cvtpk(oacc[2] * sc, oacc[3] * sc), cvtpk(oacc[4] * sc, oacc[5] * sc), cvtpk(oacc[6] * sc, oacc[7] * sc)};
#pragma unroll
      for (int e = 0; e < 4; ++e) o[e] = (o[e] == ~0u) ? 0x7FC07FC0u : o[e];
      __builtin_amdgcn_raw_buffer_store_b128(o, rs, WS_O + ((qb * 8 + w) * 32 + head) * 1024 + lane * 16, 0, AUX_SC1);
    }
    u32x4 wf[8][2], of[8];
#pragma unroll
    for (int j = 0; j < 8; ++j) {
      wf[j][0] = __builtin_amdgcn_raw_buffer_load_b128(rs, WS_WOF + ((nb * 2) * 32 + kq * 8 + j) * 1024 + lane * 16, 0, AUX_SC1);
      wf[j][1] = __builtin_amdgcn_raw_buffer_load_b128(rs, WS_WOF + ((nb * 2 + 1) * 32 + kq * 8 + j) * 1024 + lane * 16, 0, AUX_SC1);
    }
    const int ncol = nb * 64 + kq * 16;
    const u32x2 bia = *(const u32x2*)(b2 + ncol + 4 * hh), bib = *(const u32x2*)(b2 + ncol + 8 + 4 * hh);
    const float og = rbf(1.0f - gt);
    fa0 *= og; fa1 *= og;
    BARRIER();
    for (u32 spins = 0;;) {
      bool ok = true;
#pragma unroll
      for (int j = 0; j < 8; ++j) of[j] = __builtin_amdgcn_raw_buffer_load_b128(rs, WS_O + ((tb * 2 + tb2) * 32 + kq * 8 + j) * 1024 + lane * 16, 0, AUX_SC1);
#pragma unroll
      for (int j = 0; j < 8; ++j) ok = ok && of[j][0] != ~0u && of[j][1] != ~0u && of[j][2] != ~0u && of[j][3] != ~0u;
      if (__all(ok) || !alive) break;
      if (++spins > SPIN_CAP) { alive = false; break; }
      __builtin_amdgcn_s_sleep(1);
      asm volatile("" ::: "memory");
    }
    STAMP(11);
#pragma unroll
    for (int j = 0; j < 8; ++j) { fa0 = MFMA32(asbf(wf[j][0]), asbf(of[j]), fa0); fa1 = MFMA32(asbf(wf[j][1]), asbf(of[j]), fa1); }
#pragma unroll
    for (int q = 0; q < 4; ++q) {
      const f32x16& a = (q >> 1) ? fa1 : fa0;
      const int r0 = (q & 1) * 8;
      if (q != kq) {
        lds_ptr p = lds + ((tb2 * 4 + q) * 4 + kq) * 2048 + lane * 16;
        lds_st16(p, __builtin_bit_cast(u32x4, f32x4{a[r0], a[r0 + 1], a[r0 + 2], a[r0 + 3]}));
        lds_st16(p + 1024, __builtin_bit_cast(u32x4, f32x4{a[r0 + 4], a[r0 + 5], a[r0 + 6], a[r0 + 7]}));
      }
    }
    f32x4 lo, hi;
    {
      const f32x16 a = (kq >> 1) ? fa1 : fa0;
      lo = (kq & 1) ? f32x4{a[8], a[9], a[10], a[11]} : f32x4{a[0], a[1], a[2], a[3]};
      hi = (kq & 1) ? f32x4{a[12], a[13], a[14], a[15]} : f32x4{a[4], a[5], a[6], a[7]};
    }
    lo += og * f32x4{bf2f(bia[0] & 0xffffu), bf2f(bia[0] >> 16), bf2f(bia[1] & 0xffffu), bf2f(bia[1] >> 16)};
    hi += og * f32x4{bf2f(bib[0] & 0xffffu), bf2f(bib[0] >> 16), bf2f(bib[1] & 0xffffu), bf2f(bib[1] >> 16)};
    BARRIER();
#pragma unroll
    for (int s = 1; s < 4; ++s) {
      lds_ptr p = lds + ((tb2 * 4 + kq) * 4 + ((kq + s) & 3)) * 2048 + lane * 16;
      lo += __builtin_bit_cast(f32x4, lds_ld16(p));
      hi += __builtin_bit_cast(f32x4, lds_ld16(p + 1024));
    }
    *(u32x4*)(out + (tb * 64 + tb2 * 32 + r32) * DM + ncol + 8 * hh) = pair32(lo, hi);
    STAMP(12);
  }
}

extern "C" void kernel_launch(void* const* d_in, const int* in_sizes, int n_in, void* d_out, int out_size, void* d_ws, size_t ws_size,
                              hipStream_t stream) {
  const u16 *x = (const u16*)d_in[0], *wqkv = (const u16*)d_in[1], *wo = (const u16*)d_in[2], *w1 = (const u16*)d_in[4],
            *b1 = (const u16*)d_in[5], *w2 = (const u16*)d_in[6], *b2 = (const u16*)d_in[7], *gw = (const u16*)d_in[8];
  fused_kernel<0><<<256, 512, 0, stream>>>(x, wqkv, wo, w1, b1, w2, b2, gw, (u16*)d_out, (char*)d_ws);
}
```

```cpp
#include <hip/hip_runtime.h>
#include <hip/hip_bf16.h>
#include <stdint.h>

typedef unsigned short u16;
typedef unsigned int u32;
typedef unsigned long long u64;
typedef __attribute__((ext_vector_type(8))) __bf16 bf16x8;
typedef __attribute__((ext_vector_type(4))) short s16x4;
typedef __attribute__((ext_vector_type(4))) float f32x4;
typedef __attribute__((ext_vector_type(2))) float f32x2;
typedef __attribute__((ext_vector_type(16))) float f32x16;
typedef __attribute__((ext_vector_type(4))) u32 u32x4;
typedef __attribute__((ext_vector_type(2))) u32 u32x2;
#define LAS __attribute__((address_space(3)))
#define GAS __attribute__((address_space(1)))
typedef LAS char* lds_ptr;

#define MODE 0

constexpr int SEQ = 2048, DM = 512, FF = 1792;
constexpr u32 WS_EPOCH = 0, WS_FLAG1 = 4096, WS_FLAG2 = 8192, WS_G = 16384, WS_STAMP = 32768;
constexpr u32 WS_Q = 1u << 20, WS_KV = 3u << 20, WS_H = 7u << 20, WS_W2F = 14u << 20, WS_WOF = 16u << 20, WS_O = 17u << 20, WS_END = 19u << 20;
constexpr int L_X = 0, L_W = 64 * 1024, WSLOT = 14336, KVS = 2176, LDS_BYTES = 160 * 1024;
constexpr int AUX_SC1 = 16;
constexpr u32 SPIN_CAP = 1u << 15;
constexpr float QSCALE = 0.25f * 1.4426950408889634f;

#define SBAR() __builtin_amdgcn_sched_barrier(0)
#define PIN(x) asm volatile("" : "+v"(x))
#define KEEP(x) asm volatile("" :: "v"(x))
#define MFMA32(a, b, c) __builtin_amdgcn_mfma_f32_32x32x16_bf16(a, b, c, 0, 0, 0)
#define MFMA16(a, b, c) __builtin_amdgcn_mfma_f32_16x16x32_bf16(a, b, c, 0, 0, 0)
#define BARRIER() asm volatile("s_waitcnt lgkmcnt(0)\n\ts_barrier" ::: "memory")
#define RLX_AGENT __ATOMIC_RELAXED, __HIP_MEMORY_SCOPE_AGENT

#define STAMP(i) do { } while (0)

__device__ __forceinline__ float bf2f(u32 v) { return __uint_as_float(v << 16); }
__device__ __forceinline__ u32 cvtpk(float lo, float hi) {
  typedef __attribute__((ext_vector_type(2))) __bf16 bf16x2;
  bf16x2 r = {(__bf16)lo, (__bf16)hi};
  return __builtin_bit_cast(u32, r);
}
__device__ __forceinline__ float rbf(float f) { return bf2f(cvtpk(f, f) & 0xffffu); }
__device__ __forceinline__ bf16x8 asbf(u32x4 v) { return __builtin_bit_cast(bf16x8, v); }
__device__ __forceinline__ void glds16(const void* sbase, u32 voff, u32 lds_base) {
  u32 sv;
  asm volatile("s_mov_b32 %0, m0\n\ts_mov_b32 m0, %3\n\ts_nop 0\n\tglobal_load_lds_dwordx4 %1, %2\n\ts_mov_b32 m0, %0"
               : "=&s"(sv) : "v"(voff), "s"(sbase), "s"(lds_base) : "memory");
}
__device__ __forceinline__ void glds16m(const void* sbase, u32 voff, u32 lds_base, u64 mask) {
  u32 sv;
  asm volatile("s_mov_b32 %0, m0\n\ts_mov_b32 m0, %3\n\ts_mov_b64 exec, %4\n\tglobal_load_lds_dwordx4 %1, %2\n\ts_mov_b64 exec, -1\n\ts_mov_b32 m0, %0"
               : "=&s"(sv) : "v"(voff), "s"(sbase), "s"(lds_base), "s"(mask) : "memory");
}
__device__ __forceinline__ bf16x8 trfrag(lds_ptr p0, lds_ptr p1) {
  typedef short v4i16_t __attribute__((ext_vector_type(4)));
  v4i16_t a = __builtin_amdgcn_ds_read_tr16_b64_v4i16((LAS v4i16_t*)p0);
  v4i16_t b = __builtin_amdgcn_ds_read_tr16_b64_v4i16((LAS v4i16_t*)p1);
  typedef short v8i16_t __attribute__((ext_vector_type(8)));
  v8i16_t r = __builtin_shufflevector(a, b, 0, 1, 2, 3, 4, 5, 6, 7);
  return __builtin_bit_cast(bf16x8, r);
}
__device__ __forceinline__ u32x4 lds_ld16(lds_ptr p) { return *(LAS u32x4*)p; }
__device__ __forceinline__ void lds_st16(lds_ptr p, u32x4 v) { *(LAS u32x4*)p = v; }
__device__ __forceinline__ u32x4 pair16(f32x4 a, f32x4 b) {
  u32 a0 = cvtpk(a[0], a[1]), a1 = cvtpk(a[2], a[3]), b0 = cvtpk(b[0], b[1]), b1 = cvtpk(b[2], b[3]);
  auto s0 = __builtin_amdgcn_permlane16_swap(a0, b0, false, false);
  auto s1 = __builtin_amdgcn_permlane16_swap(a1, b1, false, false);
  return u32x4{s0[0], s1[0], s0[1], s1[1]};
}
__device__ __forceinline__ u32x4 pair32(f32x4 a, f32x4 b) {
  u32 a0 = cvtpk(a[0], a[1]), a1 = cvtpk(a[2], a[3]), b0 = cvtpk(b[0], b[1]), b1 = cvtpk(b[2], b[3]);
  auto s0 = __builtin_amdgcn_permlane32_swap(a0, b0, false, false);
  auto s1 = __builtin_amdgcn_permlane32_swap(a1, b1, false, false);
  return u32x4{s0[0], s1[0], s0[1], s1[1]};
}
__device__ __forceinline__ float gelu1(float x) {
  constexpr float K0 = -2.0f * 0.7978845608028654f * 1.4426950408889634f, K1 = K0 * 0.044715f;
  float z = x * __builtin_fmaf(x * x, K1, K0);
  return x * __builtin_amdgcn_rcpf(1.0f + __builtin_amdgcn_exp2f(z));
}
__device__ __forceinline__ float xhalf_max(float m) {
  auto r = __builtin_amdgcn_permlane32_swap(__float_as_uint(m), __float_as_uint(m), false, false);
  return __builtin_fmaxf(__uint_as_float(r[0]), __uint_as_float(r[1]));
}
__device__ __forceinline__ float max16(const f32x16& s) {
  float a = __builtin_fmaxf(s[0], s[1]), b = __builtin_fmaxf(s[2], s[3]);
#pragma unroll
  for (int r = 4; r < 16; r += 4) { a = __builtin_fmaxf(__builtin_fmaxf(a, s[r]), s[r + 1]); b = __builtin_fmaxf(__builtin_fmaxf(b, s[r + 2]), s[r + 3]); }
  return __builtin_fmaxf(a, b);
}
__device__ __forceinline__ f32x16 splat16(float v) { return f32x16{v, v, v, v, v, v, v, v, v, v, v, v, v, v, v, v}; }

template <int M>
__global__ void __launch_bounds__(512, 2)
fused_kernel(const u16* __restrict__ x, const u16* __restrict__ wqkv, const u16* __restrict__ wo, const u16* __restrict__ w1,
             const u16* __restrict__ b1, const u16* __restrict__ w2, const u16* __restrict__ b2, const u16* __restrict__ gw,
             u16* __restrict__ out, char* ws) {
  __shared__ __attribute__((aligned(16))) char smem[LDS_BYTES];
  const lds_ptr lds = (lds_ptr)smem;
  const u32 lds0 = (u32)(uintptr_t)lds;
  const int tid = threadIdx.x, lane = tid & 63, w = __builtin_amdgcn_readfirstlane(tid >> 6);
  const int b = blockIdx.x, xc = b & 7, bi = b >> 3;
  const int r16 = lane & 15, g4 = lane >> 4, r32 = lane & 31, hh = lane >> 5;
  const __amdgpu_buffer_rsrc_t rs = __builtin_amdgcn_make_buffer_rsrc(ws, 0, WS_END, 0x00020000);
  const int head = xc * 4 + (bi >> 3), qb = bi & 7;
  const int tb = (xc & 3) * 8 + (bi & 7), nb = (xc >> 2) * 4 + (bi >> 3);
  bool alive = true;
  u64 tag = 0;
  STAMP(0);
#define KVOFF(j) (WS_KV + head * 131072 + ((((j) * 8 + w) + qb * 16) & 127) * 1024)
#define KVLDS(j) (lds + (((j) * 8 + w) >> 1) * KVS + (w & 1) * 1024 + lane * 16)

  if constexpr (M != 2) {
    const int rb = (xc & 1) * 8 + (bi & 7), cb = (xc >> 1) * 4 + (bi >> 3);
    const int rg = w & 3, ch = w >> 2, rot = (rb + 2 * cb) & 7;
    const u32 vx = (u32)(((lane >> 3) * DM + ((lane & 7) ^ (lane >> 3)) * 8) * 2);
    const int l12 = (lane * 43) >> 9, c12 = (lane - l12 * 12) ^ ((w & 1) << 1), l14 = (lane * 37) >> 9;
    const u32 vq = (u32)((l12 * 1536 + (c12 < 8 ? cb * 64 + c12 * 8 : 1024 - 64 + cb * 32 + c12 * 8)) * 2);
    const u32 v1 = (u32)((l14 * FF + (lane - l14 * 14) * 8) * 2);
    const char* xs = (const char*)x + (rb * 128 + 16 * w) * (DM * 2);
    const char* qs = (const char*)wqkv + (8 * w) * (1536 * 2);
    const char* fs = (const char*)w1 + (((w >> 1) * 16 + (w & 1) * 4) * FF + cb * 112) * 2;
#define ISSUE(s) do { const int kc = ((s) + rot) & 7; \
      if ((s) < 8) { \
        glds16(xs + kc * 128, vx, lds0 + L_X + ((s) & 3) * 16384 + w * 2048); \
        glds16(xs + kc * 128 + 8 * DM * 2, vx, lds0 + L_X + ((s) & 3) * 16384 + w * 2048 + 1024); \
        glds16m(qs + kc * (64 * 1536 * 2), vq, lds0 + L_W + ((s) & 3) * WSLOT + w * (8 * 192), 0x0000FFFFFFFFFFFFull); \
        glds16m(qs + kc * (64 * 1536 * 2) + 4 * 1536 * 2, vq, lds0 + L_W + ((s) & 3) * WSLOT + w * (8 * 192) + 4 * 192, 0x0000FFFFFFFFFFFFull); \
      } else if ((s) < 16) { \
        glds16m(fs + kc * (64 * FF * 2), v1, lds0 + L_W + ((s) & 3) * WSLOT + w * (8 * 224), 0x00FFFFFFFFFFFFFFull); \
        glds16m(fs + kc * (64 * FF * 2) + 8 * FF * 2, v1, lds0 + L_W + ((s) & 3) * WSLOT + w * (8 * 224) + 4 * 224, 0x00FFFFFFFFFFFFFFull); \
      } } while (0)
#define WAITB(n) asm volatile("s_waitcnt vmcnt(" #n ") lgkmcnt(0)\n\ts_barrier" ::: "memory")
    ISSUE(0); ISSUE(1); ISSUE(2);
    u32x2 epoch;
    asm volatile("global_load_dwordx2 %0, %1, %2 sc1" : "=v"(epoch) : "v"(WS_EPOCH), "s"(ws) : "memory");
    STAMP(13);
    u32x4 xf[2][16];
    f32x4 aq[3][2];
#pragma unroll
    for (int t = 0; t < 3; ++t) { aq[t][0] = f32x4{}; aq[t][1] = f32x4{}; }
    const lds_ptr xr0 = lds + L_X + (rg * 32 + r16) * 128 + ((g4 ^ (r16 & 7)) * 16), xr1 = lds + L_X + (rg * 32 + r16) * 128 + (((4 + g4) ^ (r16 & 7)) * 16);
    const lds_ptr wqb = lds + L_W + (8 * g4 + (r16 >> 2)) * 192 + (r16 & 3) * 8;
    const lds_ptr wq0 = wqb + ((2 * ch) ^ (g4 & 1)) * 32, wq1 = wqb + ((2 * ch + 1) ^ (g4 & 1)) * 32, wq2 = wqb + ((4 + ch) ^ (g4 & 1)) * 32;
#define QSTAGE(s, n) do { WAITB(n); ISSUE((s) + 3); \
      _Pragma("unroll") for (int rt = 0; rt < 2; ++rt) { xf[rt][2 * (s)] = lds_ld16(xr0 + ((s) & 3) * 16384 + rt * 2048); xf[rt][2 * (s) + 1] = lds_ld16(xr1 + ((s) & 3) * 16384 + rt * 2048); } \
      _Pragma("unroll") for (int ks = 0; ks < 2; ++ks) { const int o = ((s) & 3) * WSLOT + ks * (32 * 192); \
        const bf16x8 f0 = trfrag(wq0 + o, wq0 + o + 4 * 192), f1 = trfrag(wq1 + o, wq1 + o + 4 * 192), f2 = trfrag(wq2 + o, wq2 + o + 4 * 192); \
        _Pragma("unroll") for (int rt = 0; rt < 2; ++rt) { const bf16x8 xv = asbf(xf[rt][2 * (s) + ks]); \
          aq[0][rt] = MFMA16(f0, xv, aq[0][rt]); aq[1][rt] = MFMA16(f1, xv, aq[1][rt]); aq[2][rt] = MFMA16(xv, f2, aq[2][rt]); } } } while (0)
    QSTAGE(0, 9); STAMP(1); QSTAGE(1, 9); QSTAGE(2, 9); QSTAGE(3, 8);
    PIN(epoch);
    tag = (((u64)__builtin_amdgcn_readfirstlane(epoch[1]) << 32) | (u64)__builtin_amdgcn_readfirstlane(epoch[0])) + 1;
    asm volatile("" : "+s"(tag));
    QSTAGE(4, 8); QSTAGE(5, 8); QSTAGE(6, 6); QSTAGE(7, 4);
    STAMP(2);
    {
      const int tile = rb * 4 + rg;
      const float qsc = cb < 8 ? QSCALE : 1.0f;
      const u32 qkb = (cb < 8 ? WS_Q + (u32)(((cb * 4 + 2 * ch) * 64 + tile) * 1024) : WS_KV + (u32)(((cb - 8) * 4 + 2 * ch) * 131072 + tile * 2048)) + lane * 16;
      const u32 hstride = cb < 8 ? 65536u : 131072u;
      __builtin_amdgcn_raw_buffer_store_b128(pair16(aq[0][0] * qsc, aq[0][1] * qsc), rs, qkb, 0, AUX_SC1);
      __builtin_amdgcn_raw_buffer_store_b128(pair16(aq[1][0] * qsc, aq[1][1] * qsc), rs, qkb + hstride, 0, AUX_SC1);
      __builtin_amdgcn_raw_buffer_store_b128(pair32(aq[2][0], aq[2][1]), rs, WS_KV + (cb * 2 + ch) * 131072 + tile * 2048 + 1024 + lane * 16, 0, AUX_SC1);
    }
    STAMP(3);
    f32x4 ah[4][2];
#pragma unroll
    for (int c = 0; c < 4; ++c) { ah[c][0] = f32x4{}; ah[c][1] = f32x4{}; }
    const lds_ptr w1b = lds + L_W + (16 * (g4 >> 1) + 4 * (g4 & 1) + (r16 >> 2)) * 224 + (r16 & 3) * 8 + ch * 128;
#define FSTAGE(s, n) do { WAITB(n); ISSUE((s) + 3); \
      _Pragma("unroll") for (int ks = 0; ks < 2; ++ks) { const lds_ptr p = w1b + ((s) & 3) * WSLOT + ks * (32 * 224); \
        _Pragma("unroll") for (int c = 0; c < 3; ++c) { const bf16x8 wf = trfrag(p + c * 32, p + c * 32 + 8 * 224); \
          ah[c][0] = MFMA16(wf, asbf(xf[0][2 * ((s) - 8) + ks]), ah[c][0]); ah[c][1] = MFMA16(wf, asbf(xf[1][2 * ((s) - 8) + ks]), ah[c][1]); } \
        if (ch == 0) { const bf16x8 wf = trfrag(p + 96, p + 96 + 8 * 224); \
          ah[3][0] = MFMA16(wf, asbf(xf[0][2 * ((s) - 8) + ks]), ah[3][0]); ah[3][1] = MFMA16(wf, asbf(xf[1][2 * ((s) - 8) + ks]), ah[3][1]); } } } while (0)
    FSTAGE(8, 7); FSTAGE(9, 7); FSTAGE(10, 7);
    FSTAGE(11, 4);
    if (tid == 0) __hip_atomic_store((GAS u64*)(ws + WS_FLAG1 + b * 8), tag, RLX_AGENT);
    STAMP(4);
    const int grow = rb * 128 + cb * 8 + w;
    const u32x4 gx = *(const u32x4*)(x + grow * DM + lane * 8), gv = *(const u32x4*)(gw + lane * 8);
    u32x2 b1v[4];
#pragma unroll
    for (int c = 0; c < 4; ++c) b1v[c] = *(const u32x2*)(b1 + cb * 112 + (c == 3 ? 48 : ch * 64 + c * 16) + g4 * 4);
    u32 pt[10];
    {
      const int f = b * 9 + w, fo = f - 1792;
      const bool isw2 = f < 1792;
      const u16* src = isw2 ? w2 + ((f % 112) * 16 + 8 * hh) * DM + (f / 112) * 32 + r32 : wo + ((fo & 31) * 16 + 4 * hh) * DM + (fo >> 5) * 32 + r32;
      const int hi = isw2 ? 4 * DM : 8 * DM;
#pragma unroll
      for (int j = 0; j < 4; ++j) { pt[j] = src[j * DM]; pt[4 + j] = src[hi + j * DM]; }
    }
    {
      const int f = b * 9 + 8, fo = f - 1792;
      const bool isw2 = f < 1792;
      const u16* src = isw2 ? w2 + ((f % 112) * 16 + 8 * hh + 2 * (w & 3)) * DM + (f / 112) * 32 + r32
                            : wo + ((fo & 31) * 16 + 4 * hh + 2 * (w & 1) + 4 * (w & 2)) * DM + (fo >> 5) * 32 + r32;
      pt[8] = src[0]; pt[9] = src[DM];
    }
    FSTAGE(12, 20); FSTAGE(13, 20); FSTAGE(14, 18); FSTAGE(15, 0);
    STAMP(5);
    u32x4 kv[16];
    if constexpr (M == 0) {
      const int prb = lane < 32 ? r16 : 2 * qb + (lane & 1), pcb = lane < 16 ? 8 + (head >> 2) : lane < 32 ? (head >> 1) : (head >> 2);
      const int pb = ((pcb >> 2) << 1 | (prb >> 3)) | (((pcb & 3) << 3 | (prb & 7)) << 3);
      for (u32 spins = 0;;) {
        const u64 f = __hip_atomic_load((GAS u64*)(ws + WS_FLAG1 + pb * 8), RLX_AGENT);
        if (__all(f == tag)) break;
        if (++spins > SPIN_CAP) { alive = false; break; }
        __builtin_amdgcn_s_sleep(2);
      }
      asm volatile("" ::: "memory");
      STAMP(6);
    }
    const u32 hb = WS_H + (u32)(((rb * 4 + rg) * 112 + cb * 7 + ch * 4) * 1024) + lane * 16;
#pragma unroll
    for (int c = 0; c < 4; ++c) {
      if constexpr (M == 0) {
#pragma unroll
        for (int j = 4 * c; j < 4 * c + 4; ++j) kv[j] = __builtin_amdgcn_raw_buffer_load_b128(rs, lane * 16, KVOFF(j), AUX_SC1);
      }
      SBAR();
      if (c < 3 || ch == 0) {
        const f32x4 bb = f32x4{bf2f(b1v[c][0] & 0xffffu), bf2f(b1v[c][0] >> 16), bf2f(b1v[c][1] & 0xffffu), bf2f(b1v[c][1] >> 16)};
#pragma unroll
        for (int rt = 0; rt < 2; ++rt)
#pragma unroll
          for (int r = 0; r < 4; ++r) ah[c][rt][r] = gelu1(ah[c][rt][r] + bb[r]);
        __builtin_amdgcn_raw_buffer_store_b128(pair16(ah[c][0], ah[c][1]), rs, hb + c * 1024, 0, AUX_SC1);
      }
      SBAR();
    }
    {
      float s = 0.f;
#pragma unroll
      for (int e = 0; e < 4; ++e) { s += rbf(bf2f(gx[e] & 0xffffu) * bf2f(gv[e] & 0xffffu)); s += rbf(bf2f(gx[e] >> 16) * bf2f(gv[e] >> 16)); }
#pragma unroll
      for (int o = 32; o; o >>= 1) s += __shfl_xor(s, o);
      const float z = rbf(s), g = rbf(1.0f / (1.0f + __expf(-z)));
      if (lane == 0) __builtin_amdgcn_raw_buffer_store_b32(__float_as_uint(g), rs, WS_G + grow * 4, 0, AUX_SC1);
    }
    __builtin_amdgcn_raw_buffer_store_b128(u32x4{pt[0] | (pt[1] << 16), pt[2] | (pt[3] << 16), pt[4] | (pt[5] << 16), pt[6] | (pt[7] << 16)}, rs,
                                           WS_W2F + (b * 9 + w) * 1024 + ((b * 9 + w) < 1792 ? 0 : WS_WOF - WS_W2F - 1792 * 1024) + lane * 16, 0, AUX_SC1);
    if (w < 4) __builtin_amdgcn_raw_buffer_store_b32(pt[8] | (pt[9] << 16), rs, WS_W2F + (b * 9 + 8) * 1024 + ((b * 9 + 8) < 1792 ? 0 : WS_WOF - WS_W2F - 1792 * 1024) + lane * 16 + w * 4, 0, AUX_SC1);
    __builtin_amdgcn_raw_buffer_store_b128(u32x4{~0u, ~0u, ~0u, ~0u}, rs, WS_O + ((qb * 8 + w) * 32 + head) * 1024 + lane * 16, 0, AUX_SC1);
    STAMP(7);
    if constexpr (M == 0) {
      BARRIER();
#pragma unroll
      for (int j = 0; j < 16; ++j) lds_st16(KVLDS(j), kv[j]);
    }
  }
  if constexpr (M == 2) {
#pragma unroll
    for (int j = 0; j < 16; ++j) lds_st16(KVLDS(j), __builtin_amdgcn_raw_buffer_load_b128(rs, lane * 16, KVOFF(j), AUX_SC1));
  }
  if constexpr (M != 1) {
    if ((tid & 7) < 6) { const u32 one = ((tid & 3) == 0) ? 0x3F803F80u : 0u; lds_st16(lds + (tid >> 3) * KVS + 2048 + (tid & 7) * 16, u32x4{one, one, one, one}); }
    const bf16x8 qf = asbf(__builtin_amdgcn_raw_buffer_load_b128(rs, WS_Q + (head * 64 + qb * 8 + w) * 1024 + lane * 16, 0, AUX_SC1));
    asm volatile("s_waitcnt vmcnt(0) lgkmcnt(0)\n\ts_barrier" ::: "memory");
    if constexpr (M == 0) { if (tid == 0) __hip_atomic_store((GAS u64*)(ws + WS_FLAG2 + b * 8), tag, RLX_AGENT); }
    STAMP(8);

    lds_ptr kp = lds + lane * 16;
    const bool grp2 = (r32 >= 16 && r32 < 20) || r32 >= 28;
    const int coff = 2048 + (r32 == 16 ? 0 : r32 == 20 ? 64 : grp2 ? 16 : 80);
    lds_ptr vp0 = lds + (r32 < 16 ? 1024 + hh * 256 + r32 * 16 : coff), vp1 = lds + (r32 < 16 ? 1536 + hh * 256 + r32 * 16 : coff);
    f32x16 sA, sB, oacc = splat16(0.f), cneg;
    f32x16 fa[2][2];
    fa[0][0] = splat16(0.f); fa[0][1] = splat16(0.f); fa[1][0] = splat16(0.f); fa[1][1] = splat16(0.f);
    u32x4 kA, kB, vA0, vA1, vB0, vB1, p1 = u32x4{0, 0, 0, 0};
    u32x4 sh[2][2], sw[2][2];
    {
      kA = lds_ld16(kp);
      sB = MFMA32(asbf(kA), qf, splat16(0.f));
      cneg = splat16(-xhalf_max(max16(sB)));
      PIN(cneg);
      sA = MFMA32(asbf(kA), qf, cneg);
      kA = lds_ld16(kp + KVS); vA0 = lds_ld16(vp0); vB1 = lds_ld16(vp1);
    }
    const u32 hoff = WS_H + (u32)((tb * 2 * 112 + w * 14) * 1024) + lane * 16;
    const u32 woff = WS_W2F + (u32)((nb * 2 * 112 + w * 14) * 1024) + lane * 16;
    int fk = (tb & 7) + 4 * (nb & 3); fk = fk >= 14 ? fk - 14 : fk;
#define EX(v) __builtin_amdgcn_exp2f(v)
#define TILE(SC, SN, KC, KN, VC0, VC1, VN0, VP1, u, FM, LD) do { \
      SN = MFMA32(asbf(KC), qf, cneg); KEEP(SN); \
      KN = lds_ld16(kp + ((u) + 2) * KVS); VC1 = lds_ld16(vp1 + (u) * KVS); VN0 = lds_ld16(vp0 + ((u) + 1) * KVS); \
      SBAR(); \
      SC[0] = EX(SC[0]); SC[1] = EX(SC[1]); SC[2] = EX(SC[2]); SC[3] = EX(SC[3]); PIN(SC); \
      SBAR(); \
      oacc = MFMA32(asbf(VP1), asbf(p1), oacc); KEEP(oacc); \
      SBAR(); \
      u32x4 p0; \
      SC[4] = EX(SC[4]); SC[5] = EX(SC[5]); SC[6] = EX(SC[6]); SC[7] = EX(SC[7]); p0[0] = cvtpk(SC[0], SC[1]); p0[1] = cvtpk(SC[2], SC[3]); PIN(SC); PIN(p0); \
      SBAR(); \
      FM; LD; \
      SBAR(); \
      p0[2] = cvtpk(SC[4], SC[5]); p0[3] = cvtpk(SC[6], SC[7]); PIN(p0); SBAR(); \
      SC[8] = EX(SC[8]); SC[9] = EX(SC[9]); SC[10] = EX(SC[10]); PIN(SC); \
      SBAR(); \
      oacc = MFMA32(asbf(VC0), asbf(p0), oacc); KEEP(oacc); \
      SBAR(); \
      SC[11] = EX(SC[11]); SC[12] = EX(SC[12]); SC[13] = EX(SC[13]); SC[14] = EX(SC[14]); SC[15] = EX(SC[15]); \
      p1 = u32x4{cvtpk(SC[8], SC[9]), cvtpk(SC[10], SC[11]), cvtpk(SC[12], SC[13]), cvtpk(SC[14], SC[15])}; PIN(p1); \
      SBAR(); } while (0)
#define TA(u, FM, LD) TILE(sA, sB, kA, kB, vA0, vA1, vB0, vB1, u, FM, LD)
#define TB(u, FM, LD) TILE(sB, sA, kB, kA, vB0, vB1, vA0, vA1, u, FM, LD)
#define ADV() do { kp += 8 * KVS; vp0 += 8 * KVS; vp1 += 8 * KVS; } while (0)
#define LDH(s, i, so) sh[s][i] = __builtin_amdgcn_raw_buffer_load_b128(rs, hoff, (so) + (i) * (112 * 1024), AUX_SC1)
#define LDW(s, i, so) sw[s][i] = __builtin_amdgcn_raw_buffer_load_b128(rs, woff, (so) + (i) * (112 * 1024), AUX_SC1)
#define FM(s, n, t) fa[n][t] = MFMA32(asbf(sw[s][n]), asbf(sh[s][t]), fa[n][t]); KEEP(fa[n][t])
#define NEXTK() __extension__({ const int o_ = fk * 1024; fk = fk == 13 ? 0 : fk + 1; o_; })
    TA(0, , ); TB(1, , ); TA(2, , ); TB(3, , ); TA(4, , ); TB(5, , ); TA(6, , ); TB(7, , ); ADV();
    if constexpr (M == 0) {
      for (u32 spins = 0; alive;) {
        const u32x4 f0 = __builtin_amdgcn_raw_buffer_load_b128(rs, WS_FLAG2 + lane * 32, 0, AUX_SC1);
        const u32x4 f1 = __builtin_amdgcn_raw_buffer_load_b128(rs, WS_FLAG2 + lane * 32 + 16, 0, AUX_SC1);
        const u32 lo = (u32)tag, hi = (u32)(tag >> 32);
        const bool ok = f0[0] == lo && f0[1] == hi && f0[2] == lo && f0[3] == hi && f1[0] == lo && f1[1] == hi && f1[2] == lo && f1[3] == hi;
        if (__all(ok)) break;
        if (++spins > SPIN_CAP) { alive = false; break; }
        __builtin_amdgcn_s_sleep(2);
      }
      asm volatile("" ::: "memory");
      if (b == 0 && tid == 0 && alive) __hip_atomic_store((GAS u64*)(ws + WS_EPOCH), tag, RLX_AGENT);
    }
    STAMP(9);
    { const int s0 = NEXTK(), s1 = NEXTK();
      LDH(0, 0, s0); LDW(0, 0, s0); LDW(0, 1, s0); LDH(0, 1, s0); LDH(1, 0, s1); LDW(1, 0, s1); LDW(1, 1, s1); LDH(1, 1, s1); }
    const int tb2 = w & 1, kq = w >> 1;
    const float gq = __uint_as_float(__builtin_amdgcn_raw_buffer_load_b32(rs, WS_G + (qb * 256 + w * 32 + r32) * 4, 0, AUX_SC1));
    const float gt = __uint_as_float(__builtin_amdgcn_raw_buffer_load_b32(rs, WS_G + (tb * 64 + tb2 * 32 + r32) * 4, 0, AUX_SC1));
    for (int it = 1; it < 7; ++it) {
      const int s0 = NEXTK(), s1 = NEXTK();
      TA(0, FM(0, 0, 0), ); TB(1, FM(0, 1, 0), LDH(0, 0, s0)); TA(2, FM(0, 0, 1), LDW(0, 0, s0)); TB(3, FM(0, 1, 1), LDW(0, 1, s0); LDH(0, 1, s0));
      TA(4, FM(1, 0, 0), ); TB(5, FM(1, 1, 0), LDH(1, 0, s1)); TA(6, FM(1, 0, 1), LDW(1, 0, s1)); TB(7, FM(1, 1, 1), LDW(1, 1, s1); LDH(1, 1, s1));
      ADV();
    }
    TA(0, FM(0, 0, 0), ); TB(1, FM(0, 1, 0), ); TA(2, FM(0, 0, 1), ); TB(3, FM(0, 1, 1), );
    TA(4, FM(1, 0, 0), ); TB(5, FM(1, 1, 0), ); TA(6, FM(1, 0, 1), ); TB(7, FM(1, 1, 1), );
    oacc = MFMA32(asbf(vB1), asbf(p1), oacc);
    STAMP(10);
    if (__builtin_expect(!__all(oacc[8] <= 1073741824.0f), 0)) {
      lds_ptr k2 = lds + lane * 16, v2 = vp0 - 56 * KVS, v3 = vp1 - 56 * KVS;
      float m = -INFINITY;
      for (int t = 0; t < 64; ++t) m = __builtin_fmaxf(m, max16(MFMA32(asbf(lds_ld16(k2 + t * KVS)), qf, splat16(0.f))));
      cneg = splat16(-xhalf_max(m));
      oacc = splat16(0.f);
      for (int t = 0; t < 64; ++t) {
        f32x16 s = MFMA32(asbf(lds_ld16(k2 + t * KVS)), qf, cneg);
#pragma unroll
        for (int r = 0; r < 16; ++r) s[r] = EX(s[r]);
        oacc = MFMA32(asbf(lds_ld16(v2 + t * KVS)), asbf(u32x4{cvtpk(s[0], s[1]), cvtpk(s[2], s[3]), cvtpk(s[4], s[5]), cvtpk(s[6], s[7])}), oacc);
        oacc = MFMA32(asbf(lds_ld16(v3 + t * KVS)), asbf(u32x4{cvtpk(s[8], s[9]), cvtpk(s[10], s[11]), cvtpk(s[12], s[13]), cvtpk(s[14], s[15])}), oacc);
      }
    }
    {
      const float sc = gq / oacc[8];
      u32x4 o = u32x4{cvtpk(oacc[0] * sc, oacc[1] * sc), cvtpk(oacc[2] * sc, oacc[3] * sc), cvtpk(oacc[4] * sc, oacc[5] * sc), cvtpk(oacc[6] * sc, oacc[7] * sc)};
#pragma unroll
      for (int e = 0; e < 4; ++e) o[e] = (o[e] == ~0u) ? 0x7FC07FC0u : o[e];
      __builtin_amdgcn_raw_buffer_store_b128(o, rs, WS_O + ((qb * 8 + w) * 32 + head) * 1024 + lane * 16, 0, AUX_SC1);
    }
    u32x4 wf[8][2], of[8];
#pragma unroll
    for (int j = 0; j < 8; ++j) {
      wf[j][0] = __builtin_amdgcn_raw_buffer_load_b128(rs, WS_WOF + ((nb * 2) * 32 + kq * 8 + j) * 1024 + lane * 16, 0, AUX_SC1);
      wf[j][1] = __builtin_amdgcn_raw_buffer_load_b128(rs, WS_WOF + ((nb * 2 + 1) * 32 + kq * 8 + j) * 1024 + lane * 16, 0, AUX_SC1);
    }
    const int ncol = nb * 64 + kq * 16;
    const u32x2 bia = *(const u32x2*)(b2 + ncol + 4 * hh), bib = *(const u32x2*)(b2 + ncol + 8 + 4 * hh);
    BARRIER();
#define Q4(a, r) __builtin_bit_cast(u32x4, f32x4{(a)[r], (a)[(r) + 1], (a)[(r) + 2], (a)[(r) + 3]})
#pragma unroll
    for (int d = 0; d < 8; ++d) {
      lds_ptr p = lds + (d * 8 + w) * 2048 + lane * 16;
      lds_st16(p, Q4(fa[d >> 2][d & 1], ((d >> 1) & 1) * 8));
      lds_st16(p + 1024, Q4(fa[d >> 2][d & 1], ((d >> 1) & 1) * 8 + 4));
    }
    BARRIER();
    f32x4 lo = f32x4{bf2f(bia[0] & 0xffffu), bf2f(bia[0] >> 16), bf2f(bia[1] & 0xffffu), bf2f(bia[1] >> 16)};
    f32x4 hi = f32x4{bf2f(bib[0] & 0xffffu), bf2f(bib[0] >> 16), bf2f(bib[1] & 0xffffu), bf2f(bib[1] >> 16)};
#pragma unroll
    for (int s = 0; s < 8; ++s) {
      lds_ptr p = lds + (w * 8 + s) * 2048 + lane * 16;
      lo += __builtin_bit_cast(f32x4, lds_ld16(p));
      hi += __builtin_bit_cast(f32x4, lds_ld16(p + 1024));
    }
    { const float og = rbf(1.0f - gt); lo *= og; hi *= og; }
    PIN(lo); PIN(hi);
    BARRIER();
    STAMP(14);
    for (u32 spins = 0;;) {
      bool ok = true;
#pragma unroll
      for (int j = 0; j < 8; ++j) of[j] = __builtin_amdgcn_raw_buffer_load_b128(rs, WS_O + ((tb * 2 + tb2) * 32 + kq * 8 + j) * 1024 + lane * 16, 0, AUX_SC1);
#pragma unroll
      for (int j = 0; j < 8; ++j) ok = ok && of[j][0] != ~0u && of[j][1] != ~0u && of[j][2] != ~0u && of[j][3] != ~0u;
      if (__all(ok) || !alive) break;
      if (++spins > SPIN_CAP) { alive = false; break; }
      __builtin_amdgcn_s_sleep(1);
      asm volatile("" ::: "memory");
    }
    STAMP(11);
    f32x16 pa[2] = {splat16(0.f), splat16(0.f)};
#pragma unroll
    for (int j = 0; j < 8; ++j) { pa[0] = MFMA32(asbf(wf[j][0]), asbf(of[j]), pa[0]); pa[1] = MFMA32(asbf(wf[j][1]), asbf(of[j]), pa[1]); }
#pragma unroll
    for (int q = 0; q < 4; ++q) {
      lds_ptr p = lds + ((q * 2 + tb2) * 4 + kq) * 2048 + lane * 16;
      lds_st16(p, Q4(pa[q >> 1], (q & 1) * 8));
      lds_st16(p + 1024, Q4(pa[q >> 1], (q & 1) * 8 + 4));
    }
    BARRIER();
#pragma unroll
    for (int s = 0; s < 4; ++s) {
      lds_ptr p = lds + (w * 4 + s) * 2048 + lane * 16;
      lo += __builtin_bit_cast(f32x4, lds_ld16(p));
      hi += __builtin_bit_cast(f32x4, lds_ld16(p + 1024));
    }
    *(u32x4*)(out + (tb * 64 + tb2 * 32 + r32) * DM + ncol + 8 * hh) = pair32(lo, hi);
    STAMP(12);
  }
}

extern "C" void kernel_launch(void* const* d_in, const int* in_sizes, int n_in, void* d_out, int out_size, void* d_ws, size_t ws_size,
                              hipStream_t stream) {
  const u16 *x = (const u16*)d_in[0], *wqkv = (const u16*)d_in[1], *wo = (const u16*)d_in[2], *w1 = (const u16*)d_in[4],
            *b1 = (const u16*)d_in[5], *w2 = (const u16*)d_in[6], *b2 = (const u16*)d_in[7], *gw = (const u16*)d_in[8];
  fused_kernel<0><<<256, 512, 0, stream>>>(x, wqkv, wo, w1, b1, w2, b2, gw, (u16*)d_out, (char*)d_ws);
}
```

```cpp
#include <hip/hip_runtime.h>
#include <hip/hip_bf16.h>
#include <stdint.h>

typedef unsigned short u16;
typedef unsigned int u32;
typedef unsigned long long u64;
typedef __attribute__((ext_vector_type(8))) __bf16 bf16x8;
typedef __attribute__((ext_vector_type(2))) __bf16 bf16x2;
typedef __attribute__((ext_vector_type(4))) float f32x4;
typedef __attribute__((ext_vector_type(2))) float f32x2;
typedef __attribute__((ext_vector_type(16))) float f32x16;
typedef __attribute__((ext_vector_type(4))) u32 u32x4;
typedef __attribute__((ext_vector_type(2))) u32 u32x2;
#define LAS __attribute__((address_space(3)))
#define GAS __attribute__((address_space(1)))
typedef LAS char* lds_ptr;

constexpr int DM = 512, FF = 1792;
constexpr u32 WS_EPOCH = 0, WS_FLAG1 = 4096, WS_FLAG2 = 8192, WS_G = 16384, WS_STAMP = 20u << 20;
constexpr u32 WS_Q = 1u << 20, WS_KV = 3u << 20, WS_H = 7u << 20, WS_W2F = 14u << 20, WS_WOF = 16u << 20, WS_O = 17u << 20, WS_END = 19u << 20;
constexpr int L_X = 0, L_W = 104 * 1024, WSLOT = 14336, KVS = 2176, L_CMB = 64 * KVS, LDS_BYTES = 160 * 1024;
constexpr int AUX_SC1 = 16;
constexpr u32 SPIN_CAP = 1u << 15;
constexpr float QSCALE = 0.25f * 1.4426950408889634f;

#define SBAR() __builtin_amdgcn_sched_barrier(0)
#define PIN(x) asm volatile("" : "+v"(x))
#define KEEP(x) asm volatile("" :: "v"(x))
#define MFMA32(a, b, c) __builtin_amdgcn_mfma_f32_32x32x16_bf16(a, b, c, 0, 0, 0)
#define MFMA16(a, b, c) __builtin_amdgcn_mfma_f32_16x16x32_bf16(a, b, c, 0, 0, 0)
#define BARRIER() asm volatile("s_waitcnt lgkmcnt(0)\n\ts_barrier" ::: "memory")
#define WAITB_(n) asm volatile("s_waitcnt vmcnt(" #n ") lgkmcnt(0)\n\ts_barrier" ::: "memory")
#define WAITB(n) WAITB_(n)
#define RLX_AGENT __ATOMIC_RELAXED, __HIP_MEMORY_SCOPE_AGENT
#define EX(v) __builtin_amdgcn_exp2f(v)
#define LD16(off, soff) __builtin_amdgcn_raw_buffer_load_b128(rs, off, soff, AUX_SC1)
#define ST16(v, off) __builtin_amdgcn_raw_buffer_store_b128(v, rs, off, 0, AUX_SC1)

#define STAMP(i) do { } while (0)

__device__ __forceinline__ float bf2f(u32 v) { return __uint_as_float(v << 16); }
__device__ __forceinline__ float bfhi(u32 v) { return __uint_as_float(v & 0xffff0000u); }
__device__ __forceinline__ u32 cvtpk(float lo, float hi) { bf16x2 r = {(__bf16)lo, (__bf16)hi}; return __builtin_bit_cast(u32, r); }
__device__ __forceinline__ float rbf(float f) { return bf2f(cvtpk(f, f)); }
__device__ __forceinline__ bf16x8 asbf(u32x4 v) { return __builtin_bit_cast(bf16x8, v); }
__device__ __forceinline__ void glds16(const void* sbase, u32 voff, u32 lds_base) {
  asm volatile("s_mov_b32 m0, %2\n\ts_nop 0\n\tglobal_load_lds_dwordx4 %0, %1" :: "v"(voff), "s"(sbase), "s"(lds_base) : "memory");
}
__device__ __forceinline__ void glds16c(const void* sbase, u32 voff, u32 lds_base) {
  asm volatile("s_mov_b32 m0, %2\n\ts_nop 0\n\tglobal_load_lds_dwordx4 %0, %1 sc1" :: "v"(voff), "s"(sbase), "s"(lds_base) : "memory");
}
__device__ __forceinline__ void glds16m(const void* sbase, u32 voff, u32 lds_base, u64 mask) {
  asm volatile("s_mov_b32 m0, %2\n\ts_mov_b64 exec, %3\n\tglobal_load_lds_dwordx4 %0, %1\n\ts_mov_b64 exec, -1"
               :: "v"(voff), "s"(sbase), "s"(lds_base), "s"(mask) : "memory");
}
__device__ __forceinline__ bf16x8 trfrag(lds_ptr p0, lds_ptr p1) {
  typedef short v4i16_t __attribute__((ext_vector_type(4)));
  typedef short v8i16_t __attribute__((ext_vector_type(8)));
  v4i16_t a = __builtin_amdgcn_ds_read_tr16_b64_v4i16((LAS v4i16_t*)p0);
  v4i16_t b = __builtin_amdgcn_ds_read_tr16_b64_v4i16((LAS v4i16_t*)p1);
  v8i16_t r = __builtin_shufflevector(a, b, 0, 1, 2, 3, 4, 5, 6, 7);
  return __builtin_bit_cast(bf16x8, r);
}
__device__ __forceinline__ u32x4 lds_ld16(lds_ptr p) { return *(LAS u32x4*)p; }
__device__ __forceinline__ f32x4 lds_ldf(lds_ptr p) { return *(LAS f32x4*)p; }
__device__ __forceinline__ void lds_st16(lds_ptr p, u32x4 v) { *(LAS u32x4*)p = v; }
__device__ __forceinline__ void lds_stf(lds_ptr p, f32x4 v) { *(LAS f32x4*)p = v; }
__device__ __forceinline__ u32x4 pair16(f32x4 a, f32x4 b) {
  u32 a0 = cvtpk(a[0], a[1]), a1 = cvtpk(a[2], a[3]), b0 = cvtpk(b[0], b[1]), b1 = cvtpk(b[2], b[3]);
  auto s0 = __builtin_amdgcn_permlane16_swap(a0, b0, false, false);
  auto s1 = __builtin_amdgcn_permlane16_swap(a1, b1, false, false);
  return u32x4{s0[0], s1[0], s0[1], s1[1]};
}
__device__ __forceinline__ u32x4 pair32(f32x4 a, f32x4 b) {
  u32 a0 = cvtpk(a[0], a[1]), a1 = cvtpk(a[2], a[3]), b0 = cvtpk(b[0], b[1]), b1 = cvtpk(b[2], b[3]);
  auto s0 = __builtin_amdgcn_permlane32_swap(a0, b0, false, false);
  auto s1 = __builtin_amdgcn_permlane32_swap(a1, b1, false, false);
  return u32x4{s0[0], s1[0], s0[1], s1[1]};
}
__device__ __forceinline__ f32x2 gelu2(f32x2 x) {
  constexpr float K0 = -2.0f * 0.7978845608028654f * 1.4426950408889634f, K1 = K0 * 0.044715f;
  const f32x2 z = x * __builtin_elementwise_fma(x * x, f32x2{K1, K1}, f32x2{K0, K0});
  const f32x2 d = f32x2{EX(z[0]), EX(z[1])} + 1.0f;
  return x * f32x2{__builtin_amdgcn_rcpf(d[0]), __builtin_amdgcn_rcpf(d[1])};
}
__device__ __forceinline__ f32x4 gelu4(f32x4 x) {
  const f32x2 a = gelu2(f32x2{x[0], x[1]}), c = gelu2(f32x2{x[2], x[3]});
  return f32x4{a[0], a[1], c[0], c[1]};
}
__device__ __forceinline__ f32x4 unpk4(u32x2 v) { return f32x4{bf2f(v[0]), bfhi(v[0]), bf2f(v[1]), bfhi(v[1])}; }
__device__ __forceinline__ float xhalf_max(float m) {
  auto r = __builtin_amdgcn_permlane32_swap(__float_as_uint(m), __float_as_uint(m), false, false);
  return __builtin_fmaxf(__uint_as_float(r[0]), __uint_as_float(r[1]));
}
__device__ __forceinline__ float max16(const f32x16& s) {
  float a = __builtin_fmaxf(s[0], s[1]), b = __builtin_fmaxf(s[2], s[3]);
#pragma unroll
  for (int r = 4; r < 16; r += 4) { a = __builtin_fmaxf(__builtin_fmaxf(a, s[r]), s[r + 1]); b = __builtin_fmaxf(__builtin_fmaxf(b, s[r + 2]), s[r + 3]); }
  return __builtin_fmaxf(a, b);
}
__device__ __forceinline__ f32x16 splat16(float v) { return f32x16{v, v, v, v, v, v, v, v, v, v, v, v, v, v, v, v}; }
template <int CTRL> __device__ __forceinline__ float dppadd(float s) {
  return s + __uint_as_float(__builtin_amdgcn_update_dpp(0u, __float_as_uint(s), CTRL, 0xf, 0xf, true));
}
__device__ __forceinline__ float wave_sum(float s) {
  s = dppadd<0x128>(s); s = dppadd<0x124>(s); s = dppadd<0x122>(s); s = dppadd<0x121>(s);
  auto a = __builtin_amdgcn_permlane16_swap(__float_as_uint(s), __float_as_uint(s), false, false);
  s = __uint_as_float(a[0]) + __uint_as_float(a[1]);
  auto c = __builtin_amdgcn_permlane32_swap(__float_as_uint(s), __float_as_uint(s), false, false);
  return __uint_as_float(c[0]) + __uint_as_float(c[1]);
}
__device__ __forceinline__ u32x4 pack8(const f32x16& s, int r) {
  return u32x4{cvtpk(s[r], s[r + 1]), cvtpk(s[r + 2], s[r + 3]), cvtpk(s[r + 4], s[r + 5]), cvtpk(s[r + 6], s[r + 7])};
}

__global__ void __launch_bounds__(512, 2)
fused_kernel(const u16* __restrict__ x, const u16* __restrict__ wqkv, const u16* __restrict__ wo, const u16* __restrict__ w1,
             const u16* __restrict__ b1, const u16* __restrict__ w2, const u16* __restrict__ b2, const u16* __restrict__ gw,
             u16* __restrict__ out, char* ws) {
  __shared__ __attribute__((aligned(16))) char smem[LDS_BYTES];
  const lds_ptr lds = (lds_ptr)smem;
  const u32 lds0 = (u32)(uintptr_t)lds;
  const int tid = threadIdx.x, lane = tid & 63, w = __builtin_amdgcn_readfirstlane(tid >> 6);
  const int b = blockIdx.x, xc = b & 7, bi = b >> 3;
  const int r16 = lane & 15, g4 = lane >> 4, r32 = lane & 31, hh = lane >> 5;
  const __amdgpu_buffer_rsrc_t rs = __builtin_amdgcn_make_buffer_rsrc(ws, 0, WS_END, 0x00020000);
  const int rb = (xc & 1) * 8 + (bi & 7), cb = (xc >> 1) * 4 + (bi >> 3);
  const int head = xc * 4 + (bi >> 3), qb = bi & 7;
  const int tb = (xc & 3) * 8 + (bi & 7), nb = (xc >> 2) * 4 + (bi >> 3);
  bool alive = true;
  STAMP(0);

  const int rg = w & 3, ch = w >> 2, rot = (rb + 2 * cb) & 7;
  const u32 vx = (u32)(((lane >> 3) * DM + ((lane & 7) ^ (lane >> 3)) * 8) * 2);
  const int l12 = (lane * 43) >> 9, c12 = (lane - l12 * 12) ^ ((w & 1) << 1), l14 = (lane * 37) >> 9;
  const u32 vq = (u32)((l12 * 1536 + (c12 < 8 ? cb * 64 + c12 * 8 : 1024 - 64 + cb * 32 + c12 * 8)) * 2);
  const u32 v1 = (u32)((l14 * FF + (lane - l14 * 14) * 8) * 2);
  const char* xs = (const char*)x + (rb * 128 + 16 * w) * (DM * 2);
  const char* qs = (const char*)wqkv + (8 * w) * (1536 * 2);
  const char* fs = (const char*)w1 + (((w >> 1) * 16 + (w & 1) * 4) * FF + cb * 112) * 2;
#define ISSUE_A(s) do { const int kc = ((s) + rot) & 7; \
    if ((s) < 8) { \
      glds16(xs + kc * 128, vx, lds0 + L_X + ((s) & 3) * 16384 + w * 2048); \
      glds16(xs + kc * 128 + 8 * DM * 2, vx, lds0 + L_X + ((s) & 3) * 16384 + w * 2048 + 1024); \
    } else if ((s) < 16) { \
      glds16m(fs + kc * (64 * FF * 2), v1, lds0 + L_W + ((s) & 3) * WSLOT + w * (8 * 224), 0x00FFFFFFFFFFFFFFull); \
    } } while (0)
#define ISSUE_B(s) do { const int kc = ((s) + rot) & 7; \
    if ((s) < 8) { \
      glds16m(qs + kc * (64 * 1536 * 2), vq, lds0 + L_W + ((s) & 3) * WSLOT + w * (8 * 192), 0x0000FFFFFFFFFFFFull); \
      glds16m(qs + kc * (64 * 1536 * 2) + 4 * 1536 * 2, vq, lds0 + L_W + ((s) & 3) * WSLOT + w * (8 * 192) + 4 * 192, 0x0000FFFFFFFFFFFFull); \
    } else if ((s) < 16) { \
      glds16m(fs + kc * (64 * FF * 2) + 8 * FF * 2, v1, lds0 + L_W + ((s) & 3) * WSLOT + w * (8 * 224) + 4 * 224, 0x00FFFFFFFFFFFFFFull); \
    } } while (0)
  ISSUE_A(0); ISSUE_B(0); STAMP(1); ISSUE_A(1); ISSUE_B(1); ISSUE_A(2); ISSUE_B(2);
  u32x2 epoch;
  asm volatile("global_load_dwordx2 %0, %1, %2 sc1" : "=v"(epoch) : "v"(WS_EPOCH), "s"(ws) : "memory");
  u32 warm;
  {
    const char* a = ws + (size_t)(lane < 9 ? lane : 9) * (2u << 20);
    a = lane == 16 ? (const char*)w1 : lane == 17 ? (const char*)w2 : lane == 18 ? (const char*)wo : lane == 19 ? (const char*)b1 : a;
    a = lane == 20 ? (const char*)b2 : lane == 21 ? (const char*)gw : lane == 22 ? (const char*)out : lane == 23 ? (const char*)out + (2u << 20) - 64 : a;
    a = lane == 24 ? (const char*)w1 + 512 * FF * 2 - 64 : lane == 25 ? (const char*)w2 + 512 * FF * 2 - 64 : lane == 26 ? (const char*)wo + 512 * 512 * 2 - 64 : a;
    asm volatile("global_load_dword %0, %1, off" : "=v"(warm) : "v"(a) : "memory");
  }
#define NW0 10
  STAMP(13);
  u32x4 xf[2][16];
  f32x4 aq[3][2];
  bf16x8 wa[4], wb[4];
#pragma unroll
  for (int t = 0; t < 3; ++t) { aq[t][0] = f32x4{}; aq[t][1] = f32x4{}; }
  const lds_ptr xr0 = lds + L_X + (rg * 32 + r16) * 128 + ((g4 ^ (r16 & 7)) * 16), xr1 = lds + L_X + (rg * 32 + r16) * 128 + (((4 + g4) ^ (r16 & 7)) * 16);
  const lds_ptr wqb = lds + L_W + (8 * g4 + (r16 >> 2)) * 192 + (r16 & 3) * 8;
  const lds_ptr wq0 = wqb + ((2 * ch) ^ (g4 & 1)) * 32, wq1 = wqb + ((2 * ch + 1) ^ (g4 & 1)) * 32, wq2 = wqb + ((4 + ch) ^ (g4 & 1)) * 32;
#define QRD(F, s, ks) do { const int o = ((s) & 3) * WSLOT + (ks) * (32 * 192); \
    F[0] = trfrag(wq0 + o, wq0 + o + 4 * 192); F[1] = trfrag(wq1 + o, wq1 + o + 4 * 192); F[2] = trfrag(wq2 + o, wq2 + o + 4 * 192); } while (0)
#define QMM(F, kk, rt) do { const bf16x8 xv = asbf(xf[rt][kk]); \
    aq[0][rt] = MFMA16(F[0], xv, aq[0][rt]); aq[1][rt] = MFMA16(F[1], xv, aq[1][rt]); aq[2][rt] = MFMA16(xv, F[2], aq[2][rt]); \
    KEEP(aq[0][rt]); KEEP(aq[1][rt]); KEEP(aq[2][rt]); SBAR(); } while (0)
#define QSTAGE(s, n) do { WAITB(n); STAMP(16 + (s)); \
    xf[0][2 * (s)] = lds_ld16(xr0 + ((s) & 3) * 16384); xf[1][2 * (s)] = lds_ld16(xr0 + ((s) & 3) * 16384 + 2048); QRD(wa, s, 0); SBAR(); \
    if ((s) > 0) QMM(wb, 2 * (s) - 1, 0); \
    ISSUE_A((s) + 3); SBAR(); \
    if ((s) > 0) QMM(wb, 2 * (s) - 1, 1); \
    xf[0][2 * (s) + 1] = lds_ld16(xr1 + ((s) & 3) * 16384); xf[1][2 * (s) + 1] = lds_ld16(xr1 + ((s) & 3) * 16384 + 2048); QRD(wb, s, 1); SBAR(); \
    QMM(wa, 2 * (s), 0); \
    ISSUE_B((s) + 3); SBAR(); \
    QMM(wa, 2 * (s), 1); } while (0)
  QSTAGE(0, NW0); QSTAGE(1, NW0); QSTAGE(2, NW0); QSTAGE(3, 8);
  PIN(epoch);
  PIN(warm);
  u64 tag = (((u64)__builtin_amdgcn_readfirstlane(epoch[1]) << 32) | (u64)__builtin_amdgcn_readfirstlane(epoch[0])) + 1;
  asm volatile("" : "+s"(tag));
  QSTAGE(4, 8); QSTAGE(5, 8); QSTAGE(6, 6); QSTAGE(7, 4);
  QMM(wb, 15, 0); QMM(wb, 15, 1);
  STAMP(2);
  {
    const int tile = rb * 4 + rg;
    const float qsc = cb < 8 ? QSCALE : 1.0f;
    const u32 qkb = (cb < 8 ? WS_Q + (u32)(((cb * 4 + 2 * ch) * 64 + tile) * 1024) : WS_KV + (u32)(((cb - 8) * 4 + 2 * ch) * 131072 + tile * 2048)) + lane * 16;
    const u32 hstride = cb < 8 ? 65536u : 131072u;
    ST16(pair16(aq[0][0] * qsc, aq[0][1] * qsc), qkb);
    ST16(pair16(aq[1][0] * qsc, aq[1][1] * qsc), qkb + hstride);
    ST16(pair32(aq[2][0], aq[2][1]), WS_KV + (cb * 2 + ch) * 131072 + tile * 2048 + 1024 + lane * 16);
  }
  STAMP(3);
  f32x4 ah[4][2];
#pragma unroll
  for (int c = 0; c < 4; ++c) { ah[c][0] = f32x4{}; ah[c][1] = f32x4{}; }
  const lds_ptr w1b = lds + L_W + (16 * (g4 >> 1) + 4 * (g4 & 1) + (r16 >> 2)) * 224 + (r16 & 3) * 8 + ch * 128;
#define FRD(F, s, ks) do { const lds_ptr p = w1b + ((s) & 3) * WSLOT + (ks) * (32 * 224); \
    F[0] = trfrag(p, p + 8 * 224); F[1] = trfrag(p + 32, p + 32 + 8 * 224); F[2] = trfrag(p + 64, p + 64 + 8 * 224); \
    if (ch == 0) F[3] = trfrag(p + 96, p + 96 + 8 * 224); } while (0)
#define FMM(F, kk, rt) do { const bf16x8 xv = asbf(xf[rt][kk]); \
    ah[0][rt] = MFMA16(F[0], xv, ah[0][rt]); ah[1][rt] = MFMA16(F[1], xv, ah[1][rt]); ah[2][rt] = MFMA16(F[2], xv, ah[2][rt]); \
    KEEP(ah[0][rt]); KEEP(ah[1][rt]); KEEP(ah[2][rt]); \
    if (ch == 0) { ah[3][rt] = MFMA16(F[3], xv, ah[3][rt]); KEEP(ah[3][rt]); } SBAR(); } while (0)
#define KVDMA(j) do { const int sl_ = ((j) >> 1) * 4 + (w >> 1) + 32 * ((j) & 1); \
    glds16c(ws + WS_KV + head * 131072 + ((sl_ + qb * 8) & 63) * 2048 + (w & 1) * 1024, lane * 16, lds0 + sl_ * KVS + (w & 1) * 1024); } while (0)
#define FSTAGE(s, n, MID, END) do { WAITB(n); STAMP(16 + (s)); \
    FRD(wa, s, 0); SBAR(); \
    if ((s) > 8) FMM(wb, 2 * ((s) - 8) - 1, 0); \
    ISSUE_A((s) + 3); SBAR(); \
    if ((s) > 8) FMM(wb, 2 * ((s) - 8) - 1, 1); \
    FRD(wb, s, 1); SBAR(); \
    FMM(wa, 2 * ((s) - 8), 0); \
    ISSUE_B((s) + 3); MID; SBAR(); \
    FMM(wa, 2 * ((s) - 8), 1); \
    END; } while (0)
  FSTAGE(8, 7, , ); FSTAGE(9, 7, , );
  FSTAGE(10, 4, , );
  if (tid == 0) __hip_atomic_store((GAS u64*)(ws + WS_FLAG1 + b * 8), tag, RLX_AGENT);
  STAMP(4);
  FSTAGE(11, 4, , );
  const int grow = rb * 128 + cb * 8 + w;
  u32x4 gx = *(const u32x4*)(x + grow * DM + lane * 8), gv = *(const u32x4*)(gw + lane * 8);
  u32x2 b1v[4];
#pragma unroll
  for (int c = 0; c < 4; ++c) b1v[c] = *(const u32x2*)(b1 + cb * 112 + (c == 3 ? 48 : ch * 64 + c * 16) + g4 * 4);
  u32 pt[10];
  {
    const int f = b * 9 + w, fo = f - 1792;
    const bool isw2 = f < 1792;
    const u16* src = isw2 ? w2 + ((f % 112) * 16 + 8 * hh) * DM + (f / 112) * 32 + r32 : wo + ((fo & 31) * 16 + 4 * hh) * DM + (fo >> 5) * 32 + r32;
    const int hi = isw2 ? 4 * DM : 8 * DM;
#pragma unroll
    for (int j = 0; j < 4; ++j) { pt[j] = src[j * DM]; pt[4 + j] = src[hi + j * DM]; }
  }
  {
    const int f = b * 9 + 8, fo = f - 1792;
    const bool isw2 = f < 1792;
    const u16* src = isw2 ? w2 + ((f % 112) * 16 + 8 * hh + 2 * (w & 3)) * DM + (f / 112) * 32 + r32
                          : wo + ((fo & 31) * 16 + 4 * hh + 2 * (w & 1) + 4 * (w & 2)) * DM + (fo >> 5) * 32 + r32;
    pt[8] = src[0]; pt[9] = src[DM];
  }
  const int prb = lane < 32 ? r16 : 2 * qb + (lane & 1), pcb = lane < 16 ? 8 + (head >> 2) : lane < 32 ? (head >> 1) : (head >> 2);
  const u32 f1off = WS_FLAG1 + (((pcb >> 2) << 1 | (prb >> 3)) | (((pcb & 3) << 3 | (prb & 7)) << 3)) * 8;
  const int qg = w & 3, kh = w >> 2;
  u64 f1v;
  u32x4 qAu, qBu;
#define F1LOAD() asm volatile("global_load_dwordx2 %0, %1, %2 sc1" : "=v"(f1v) : "v"(f1off), "s"(ws) : "memory")
#define F1CHECK() do { \
    asm volatile("s_waitcnt vmcnt(0)" : "+v"(f1v) :: "memory"); \
    asm volatile("" : "+v"(gx), "+v"(gv), "+v"(b1v[0]), "+v"(b1v[1]), "+v"(b1v[2]), "+v"(b1v[3]), "+v"(pt[8]), "+v"(pt[9])); \
    asm volatile("" : "+v"(pt[0]), "+v"(pt[1]), "+v"(pt[2]), "+v"(pt[3]), "+v"(pt[4]), "+v"(pt[5]), "+v"(pt[6]), "+v"(pt[7])); \
    for (u32 spins = 0; !__all(f1v == tag);) { \
      if (++spins > SPIN_CAP) { alive = false; break; } \
      __builtin_amdgcn_s_sleep(1); \
      f1v = __hip_atomic_load((GAS u64*)(ws + f1off), RLX_AGENT); } \
    asm volatile("" ::: "memory"); STAMP(6); \
    const u32 qo = WS_Q + (head * 64 + qb * 8 + qg * 2) * 1024 + lane * 16; \
    asm volatile("global_load_dwordx4 %0, %2, %3 sc1\n\tglobal_load_dwordx4 %1, %2, %3 offset:1024 sc1" : "=&v"(qAu), "=&v"(qBu) : "v"(qo), "s"(ws) : "memory"); \
    KVDMA(0); KVDMA(1); } while (0)
  FSTAGE(12, 20, , F1LOAD());
  FSTAGE(13, 21, , F1CHECK());
  FSTAGE(14, 63, KVDMA(2); KVDMA(3), KVDMA(4); KVDMA(5));
  FSTAGE(15, 63, KVDMA(6); KVDMA(7), KVDMA(8); KVDMA(10));
  FMM(wb, 15, 0); FMM(wb, 15, 1);
  STAMP(5);
  BARRIER();
  KVDMA(12); KVDMA(14);
  if ((tid & 7) < 6) { const u32 one = ((tid & 3) == 0) ? 0x3F803F80u : 0u; lds_st16(lds + (tid >> 3) * KVS + 2048 + (tid & 7) * 16, u32x4{one, one, one, one}); }
  {
    const u32 hb = WS_H + (u32)(((rb * 4 + rg) * 112 + cb * 7 + ch * 4) * 1024) + lane * 16;
#pragma unroll
    for (int c = 0; c < 4; ++c) {
      if (c < 3 || ch == 0) {
        const f32x4 bb = unpk4(b1v[c]);
        ST16(pair16(gelu4(ah[c][0] + bb), gelu4(ah[c][1] + bb)), hb + c * 1024);
      }
      if (c == 0) { KVDMA(9); KVDMA(11); }
      if (c == 1) { KVDMA(13); KVDMA(15); }
    }
    float s = 0.f;
#pragma unroll
    for (int e = 0; e < 4; ++e) { const u32 ua = gx[e], ub = gv[e]; s = __builtin_amdgcn_fdot2_f32_bf16(__builtin_bit_cast(bf16x2, ua), __builtin_bit_cast(bf16x2, ub), s, false); }
    const float z = rbf(wave_sum(s)), g = rbf(__builtin_amdgcn_rcpf(1.0f + EX(z * -1.4426950408889634f)));
    if (lane == 0) __builtin_amdgcn_raw_buffer_store_b32(__float_as_uint(g), rs, WS_G + grow * 4, 0, AUX_SC1);
    ST16((u32x4{pt[0] | (pt[1] << 16), pt[2] | (pt[3] << 16), pt[4] | (pt[5] << 16), pt[6] | (pt[7] << 16)}),
         WS_W2F + (b * 9 + w) * 1024 + ((b * 9 + w) < 1792 ? 0 : WS_WOF - WS_W2F - 1792 * 1024) + lane * 16);
    if (w < 4) __builtin_amdgcn_raw_buffer_store_b32(pt[8] | (pt[9] << 16), rs, WS_W2F + (b * 9 + 8) * 1024 + ((b * 9 + 8) < 1792 ? 0 : WS_WOF - WS_W2F - 1792 * 1024) + lane * 16 + w * 4, 0, AUX_SC1);
    ST16((u32x4{~0u, ~0u, ~0u, ~0u}), WS_O + ((qb * 8 + w) * 32 + head) * 1024 + lane * 16);
  }
  STAMP(7);
  asm volatile("s_waitcnt vmcnt(14) lgkmcnt(0)\n\ts_barrier" : "+v"(qAu), "+v"(qBu) :: "memory");
  const bf16x8 qA = asbf(qAu), qB = asbf(qBu);
  STAMP(8);

  lds_ptr kp = lds + kh * (32 * KVS) + lane * 16;
  const bool grp2 = (r32 >= 16 && r32 < 20) || r32 >= 28;
  const int coff = 2048 + (r32 == 16 ? 0 : r32 == 20 ? 64 : grp2 ? 16 : 80);
  lds_ptr vp0 = lds + kh * (32 * KVS) + (r32 < 16 ? 1024 + hh * 256 + r32 * 16 : coff), vp1 = lds + kh * (32 * KVS) + (r32 < 16 ? 1536 + hh * 256 + r32 * 16 : coff);
  f32x16 sA, sB, oA = splat16(0.f), oB = splat16(0.f), cn16;
  f32x16 fa[2][2];
  fa[0][0] = splat16(0.f); fa[0][1] = splat16(0.f); fa[1][0] = splat16(0.f); fa[1][1] = splat16(0.f);
  u32x4 kf, v0, v1f, p1 = u32x4{0, 0, 0, 0};
  u32x4 sh[2][2], sw[2][2];
  {
    kf = lds_ld16(kp); v0 = lds_ld16(vp0); v1f = lds_ld16(vp1);
    sA = MFMA32(asbf(kf), qA, splat16(0.f));
    sB = MFMA32(asbf(kf), qB, splat16(0.f));
    cn16 = splat16(-xhalf_max(__builtin_fmaxf(max16(sA), max16(sB))));
    PIN(cn16);
    sA = MFMA32(asbf(kf), qA, cn16);
  }
  const u32 hoff = WS_H + (u32)((tb * 2 * 112 + w * 14) * 1024) + lane * 16;
  const u32 woff = WS_W2F + (u32)((nb * 2 * 112 + w * 14) * 1024) + lane * 16;
  int fk = (tb & 7) + 4 * (nb & 3); fk = fk >= 14 ? fk - 14 : fk;
#define UNIT(SC, SN, QN, OC, OP, RD0, RD1, RD2, FMX, LD) do { \
    SN = MFMA32(asbf(kf), QN, cn16); KEEP(SN); RD0; \
    SBAR(); \
    SC[0] = EX(SC[0]); SC[1] = EX(SC[1]); SC[2] = EX(SC[2]); SC[3] = EX(SC[3]); PIN(SC); \
    SBAR(); \
    OP = MFMA32(asbf(v1f), asbf(p1), OP); KEEP(OP); RD1; \
    SBAR(); \
    u32x4 p0; \
    SC[4] = EX(SC[4]); SC[5] = EX(SC[5]); SC[6] = EX(SC[6]); SC[7] = EX(SC[7]); p0[0] = cvtpk(SC[0], SC[1]); p0[1] = cvtpk(SC[2], SC[3]); PIN(SC); PIN(p0); \
    SBAR(); \
    FMX; LD; \
    SBAR(); \
    p0[2] = cvtpk(SC[4], SC[5]); p0[3] = cvtpk(SC[6], SC[7]); PIN(p0); SBAR(); \
    SC[8] = EX(SC[8]); SC[9] = EX(SC[9]); SC[10] = EX(SC[10]); PIN(SC); \
    SBAR(); \
    OC = MFMA32(asbf(v0), asbf(p0), OC); KEEP(OC); RD2; \
    SBAR(); \
    SC[11] = EX(SC[11]); SC[12] = EX(SC[12]); SC[13] = EX(SC[13]); SC[14] = EX(SC[14]); SC[15] = EX(SC[15]); \
    p1 = pack8(SC, 8); PIN(p1); \
    SBAR(); } while (0)
#define UA(t, FMX, LD) UNIT(sA, sB, qB, oA, oB, kf = lds_ld16(kp + ((t) + 1) * KVS), v1f = lds_ld16(vp1 + (t) * KVS), , FMX, LD)
#define UB(t, FMX, LD) UNIT(sB, sA, qA, oB, oA, , , v0 = lds_ld16(vp0 + ((t) + 1) * KVS), FMX, LD)
#define ADV() do { kp += 4 * KVS; vp0 += 4 * KVS; vp1 += 4 * KVS; } while (0)
#define LDH(s, i, so) sh[s][i] = LD16(hoff, (so) + (i) * (112 * 1024))
#define LDW(s, i, so) sw[s][i] = LD16(woff, (so) + (i) * (112 * 1024))
#define FM(s, n, t) fa[n][t] = MFMA32(asbf(sw[s][n]), asbf(sh[s][t]), fa[n][t]); KEEP(fa[n][t])
#define NEXTK() __extension__({ const int o_ = fk * 1024; fk = fk == 13 ? 0 : fk + 1; o_; })
  UA(0, , ); UB(0, , ); UA(1, , ); UB(1, , );
  WAITB(0);
  if (tid == 0) __hip_atomic_store((GAS u64*)(ws + WS_FLAG2 + b * 8), tag, RLX_AGENT);
  UA(2, , ); UB(2, , ); UA(3, , ); UB(3, , ); ADV();
  u32x4 f0, f1;
  UA(0, , ); UB(0, , f0 = LD16(WS_FLAG2 + lane * 32, 0); f1 = LD16(WS_FLAG2 + lane * 32 + 16, 0)); UA(1, , ); UB(1, , );
  for (u32 spins = 0; alive;) {
    const u32 lo = (u32)tag, hi = (u32)(tag >> 32);
    const bool ok = f0[0] == lo && f0[1] == hi && f0[2] == lo && f0[3] == hi && f1[0] == lo && f1[1] == hi && f1[2] == lo && f1[3] == hi;
    if (__all(ok)) break;
    if (++spins > SPIN_CAP) { alive = false; break; }
    __builtin_amdgcn_s_sleep(1);
    f0 = LD16(WS_FLAG2 + lane * 32, 0); f1 = LD16(WS_FLAG2 + lane * 32 + 16, 0);
  }
  asm volatile("" ::: "memory");
  if (b == 0 && tid == 0 && alive) __hip_atomic_store((GAS u64*)(ws + WS_EPOCH), tag, RLX_AGENT);
  STAMP(9);
  { const int s0 = NEXTK(), s1 = NEXTK();
    LDH(0, 0, s0); LDW(0, 0, s0); LDW(0, 1, s0); LDH(0, 1, s0); LDH(1, 0, s1); LDW(1, 0, s1); LDW(1, 1, s1); LDH(1, 1, s1); }
  const int tb2 = w & 1, kq = w >> 1;
  const float gq = __uint_as_float(__builtin_amdgcn_raw_buffer_load_b32(rs, WS_G + (qb * 256 + qg * 64 + kh * 32 + r32) * 4, 0, AUX_SC1));
  const float gt = __uint_as_float(__builtin_amdgcn_raw_buffer_load_b32(rs, WS_G + (tb * 64 + tb2 * 32 + r32) * 4, 0, AUX_SC1));
  UA(2, , ); UB(2, , ); UA(3, , ); UB(3, , ); ADV();
  for (int it = 2; it < 8; ++it) {
    const int s0 = NEXTK(), s1 = NEXTK();
    UA(0, FM(0, 0, 0), ); UB(0, FM(0, 1, 0), LDH(0, 0, s0)); UA(1, FM(0, 0, 1), LDW(0, 0, s0)); UB(1, FM(0, 1, 1), LDW(0, 1, s0); LDH(0, 1, s0));
    UA(2, FM(1, 0, 0), ); UB(2, FM(1, 1, 0), LDH(1, 0, s1)); UA(3, FM(1, 0, 1), LDW(1, 0, s1)); UB(3, FM(1, 1, 1), LDW(1, 1, s1); LDH(1, 1, s1));
    ADV();
  }
  oB = MFMA32(asbf(v1f), asbf(p1), oB);
  STAMP(10);
  float cA = cn16[0], cB = cA;
  if (__builtin_expect(!__all(oA[8] <= 0x1p30f && oB[8] <= 0x1p30f && oA[8] >= 0x1p-60f && oB[8] >= 0x1p-60f), 0)) {
    const lds_ptr k2 = kp - 32 * KVS, v2 = vp0 - 32 * KVS, v3 = vp1 - 32 * KVS;
#define REDO(Q, O, C) do { float m = -INFINITY; \
      for (int t = 0; t < 32; ++t) m = __builtin_fmaxf(m, max16(MFMA32(asbf(lds_ld16(k2 + t * KVS)), Q, splat16(0.f)))); \
      C = -xhalf_max(m); cn16 = splat16(C); O = splat16(0.f); \
      for (int t = 0; t < 32; ++t) { \
        f32x16 s = MFMA32(asbf(lds_ld16(k2 + t * KVS)), Q, cn16); \
        _Pragma("unroll") for (int r = 0; r < 16; ++r) s[r] = EX(s[r]); \
        O = MFMA32(asbf(lds_ld16(v2 + t * KVS)), asbf(pack8(s, 0)), O); O = MFMA32(asbf(lds_ld16(v3 + t * KVS)), asbf(pack8(s, 8)), O); } } while (0)
    REDO(qA, oA, cA); REDO(qB, oB, cB);
  }
  const int ncol = nb * 64 + kq * 16;
  const u32x2 bia = *(const u32x2*)(b2 + ncol + 4 * hh), bib = *(const u32x2*)(b2 + ncol + 8 + 4 * hh);
  {
    const f32x16 os = kh ? oA : oB;
    const float cs = kh ? cA : cB;
    const lds_ptr p = lds + L_CMB + w * 2560 + lane * 16;
    lds_stf(p, f32x4{os[0], os[1], os[2], os[3]}); lds_stf(p + 1024, f32x4{os[4], os[5], os[6], os[7]});
    *(LAS f32x2*)(lds + L_CMB + w * 2560 + 2048 + lane * 8) = f32x2{os[8], cs};
  }
  BARRIER();
  {
    const f32x16 om = kh ? oB : oA;
    const float cm = kh ? cB : cA;
    const lds_ptr p = lds + L_CMB + (w ^ 4) * 2560 + lane * 16;
    const f32x4 a = lds_ldf(p), c = lds_ldf(p + 1024);
    const f32x2 lc = *(LAS f32x2*)(lds + L_CMB + (w ^ 4) * 2560 + 2048 + lane * 8);
    const float cn = __builtin_fminf(cm, lc[1]);
    float e0 = EX(cn - cm), e1 = EX(cn - lc[1]);
    const float sc = gq * __builtin_amdgcn_rcpf(om[8] * e0 + lc[0] * e1);
    e0 *= sc; e1 *= sc;
    u32x4 o = u32x4{cvtpk(om[0] * e0 + a[0] * e1, om[1] * e0 + a[1] * e1), cvtpk(om[2] * e0 + a[2] * e1, om[3] * e0 + a[3] * e1),
                    cvtpk(om[4] * e0 + c[0] * e1, om[5] * e0 + c[1] * e1), cvtpk(om[6] * e0 + c[2] * e1, om[7] * e0 + c[3] * e1)};
#pragma unroll
    for (int e = 0; e < 4; ++e) o[e] = (o[e] == ~0u) ? 0x7FC07FC0u : o[e];
    ST16(o, WS_O + ((qb * 8 + qg * 2 + kh) * 32 + head) * 1024 + lane * 16);
  }
  STAMP(14);
  SBAR();
  u32x4 wf[8][2], of[8];
#define OLOAD() _Pragma("unroll") for (int j = 0; j < 8; ++j) of[j] = LD16(WS_O + ((tb * 2 + tb2) * 32 + kq * 8 + j) * 1024 + lane * 16, 0)
#pragma unroll
  for (int j = 0; j < 8; ++j) {
    wf[j][0] = LD16(WS_WOF + ((nb * 2) * 32 + kq * 8 + j) * 1024 + lane * 16, 0);
    wf[j][1] = LD16(WS_WOF + ((nb * 2 + 1) * 32 + kq * 8 + j) * 1024 + lane * 16, 0);
  }
  FM(0, 0, 0); FM(0, 1, 0); FM(0, 0, 1); FM(0, 1, 1); FM(1, 0, 0); FM(1, 1, 0); FM(1, 0, 1); FM(1, 1, 1);
#define Q4(a, r) f32x4{(a)[r], (a)[(r) + 1], (a)[(r) + 2], (a)[(r) + 3]}
#pragma unroll
  for (int d = 0; d < 8; ++d) {
    const lds_ptr p = lds + (d * 8 + w) * 2048 + lane * 16;
    lds_stf(p, Q4(fa[d >> 2][d & 1], ((d >> 1) & 1) * 8));
    lds_stf(p + 1024, Q4(fa[d >> 2][d & 1], ((d >> 1) & 1) * 8 + 4));
  }
  OLOAD();
  BARRIER();
  f32x4 lo = unpk4(bia), hi = unpk4(bib);
#pragma unroll
  for (int s = 0; s < 8; ++s) {
    const lds_ptr p = lds + (w * 8 + s) * 2048 + lane * 16;
    lo += lds_ldf(p); hi += lds_ldf(p + 1024);
  }
  { const float og = rbf(1.0f - gt); lo *= og; hi *= og; }
  PIN(lo); PIN(hi);
  BARRIER();
  for (u32 spins = 0;;) {
    bool ok = true;
#pragma unroll
    for (int j = 0; j < 8; ++j) ok = ok && of[j][0] != ~0u && of[j][1] != ~0u && of[j][2] != ~0u && of[j][3] != ~0u;
    if (__all(ok) || !alive) break;
    if (++spins > SPIN_CAP) { alive = false; break; }
    asm volatile("" ::: "memory");
    OLOAD();
  }
  STAMP(11);
  f32x16 pa[2] = {splat16(0.f), splat16(0.f)};
#pragma unroll
  for (int j = 0; j < 8; ++j) { pa[0] = MFMA32(asbf(wf[j][0]), asbf(of[j]), pa[0]); pa[1] = MFMA32(asbf(wf[j][1]), asbf(of[j]), pa[1]); }
#pragma unroll
  for (int q = 0; q < 4; ++q) {
    const lds_ptr p = lds + ((q * 2 + tb2) * 4 + kq) * 2048 + lane * 16;
    lds_stf(p, Q4(pa[q >> 1], (q & 1) * 8));
    lds_stf(p + 1024, Q4(pa[q >> 1], (q & 1) * 8 + 4));
  }
  BARRIER();
#pragma unroll
  for (int s = 0; s < 4; ++s) {
    const lds_ptr p = lds + (w * 4 + s) * 2048 + lane * 16;
    lo += lds_ldf(p); hi += lds_ldf(p + 1024);
  }
  *(u32x4*)(out + (tb * 64 + tb2 * 32 + r32) * DM + ncol + 8 * hh) = pair32(lo, hi);
  STAMP(12);
}

extern "C" void kernel_launch(void* const* d_in, const int* in_sizes, int n_in, void* d_out, int out_size, void* d_ws, size_t ws_size,
                              hipStream_t stream) {
  const u16 *x = (const u16*)d_in[0], *wqkv = (const u16*)d_in[1], *wo = (const u16*)d_in[2], *w1 = (const u16*)d_in[4],
            *b1 = (const u16*)d_in[5], *w2 = (const u16*)d_in[6], *b2 = (const u16*)d_in[7], *gw = (const u16*)d_in[8];
  fused_kernel<<<256, 512, 0, stream>>>(x, wqkv, wo, w1, b1, w2, b2, gw, (u16*)d_out, (char*)d_ws);
}
```

```cpp
#define NO_TLBWARM
#include <hip/hip_runtime.h>
#include <hip/hip_bf16.h>
#include <stdint.h>

typedef unsigned short u16;
typedef unsigned int u32;
typedef unsigned long long u64;
typedef __attribute__((ext_vector_type(8))) __bf16 bf16x8;
typedef __attribute__((ext_vector_type(2))) __bf16 bf16x2;
typedef __attribute__((ext_vector_type(4))) float f32x4;
typedef __attribute__((ext_vector_type(2))) float f32x2;
typedef __attribute__((ext_vector_type(16))) float f32x16;
typedef __attribute__((ext_vector_type(4))) u32 u32x4;
typedef __attribute__((ext_vector_type(2))) u32 u32x2;
#define LAS __attribute__((address_space(3)))
#define GAS __attribute__((address_space(1)))
typedef LAS char* lds_ptr;

constexpr int DM = 512, FF = 1792;
constexpr u32 WS_EPOCH = 0, WS_FLAG1 = 4096, WS_FLAG2 = 8192, WS_G = 16384, WS_STAMP = 20u << 20;
constexpr u32 WS_Q = 1u << 20, WS_KV = 3u << 20, WS_H = 7u << 20, WS_W2F = 14u << 20, WS_WOF = 16u << 20, WS_O = 17u << 20, WS_END = 19u << 20;
constexpr int L_X = 0, L_W = 104 * 1024, WSLOT = 14336, KVS = 2176, L_CMB = 64 * KVS, LDS_BYTES = 160 * 1024;
constexpr int AUX_SC1 = 16;
constexpr u32 SPIN_CAP = 1u << 15;
constexpr float QSCALE = 0.25f * 1.4426950408889634f;

#define SBAR() __builtin_amdgcn_sched_barrier(0)
#define PIN(x) asm volatile("" : "+v"(x))
#define KEEP(x) asm volatile("" :: "v"(x))
#define MFMA32(a, b, c) __builtin_amdgcn_mfma_f32_32x32x16_bf16(a, b, c, 0, 0, 0)
#define MFMA16(a, b, c) __builtin_amdgcn_mfma_f32_16x16x32_bf16(a, b, c, 0, 0, 0)
#define BARRIER() asm volatile("s_waitcnt lgkmcnt(0)\n\ts_barrier" ::: "memory")
#define WAITB_(n) asm volatile("s_waitcnt vmcnt(" #n ") lgkmcnt(0)\n\ts_barrier" ::: "memory")
#define WAITB(n) WAITB_(n)
#define RLX_AGENT __ATOMIC_RELAXED, __HIP_MEMORY_SCOPE_AGENT
#define EX(v) __builtin_amdgcn_exp2f(v)
#define LD16(off, soff) __builtin_amdgcn_raw_buffer_load_b128(rs, off, soff, AUX_SC1)
#define ST16(v, off) __builtin_amdgcn_raw_buffer_store_b128(v, rs, off, 0, AUX_SC1)

#define STAMP(i) do { } while (0)

__device__ __forceinline__ float bf2f(u32 v) { return __uint_as_float(v << 16); }
__device__ __forceinline__ float bfhi(u32 v) { return __uint_as_float(v & 0xffff0000u); }
__device__ __forceinline__ u32 cvtpk(float lo, float hi) { bf16x2 r = {(__bf16)lo, (__bf16)hi}; return __builtin_bit_cast(u32, r); }
__device__ __forceinline__ float rbf(float f) { return bf2f(cvtpk(f, f)); }
__device__ __forceinline__ bf16x8 asbf(u32x4 v) { return __builtin_bit_cast(bf16x8, v); }
__device__ __forceinline__ void glds16(const void* sbase, u32 voff, u32 lds_base) {
  asm volatile("s_mov_b32 m0, %2\n\ts_nop 0\n\tglobal_load_lds_dwordx4 %0, %1" :: "v"(voff), "s"(sbase), "s"(lds_base) : "memory");
}
__device__ __forceinline__ void glds16c(const void* sbase, u32 voff, u32 lds_base) {
  asm volatile("s_mov_b32 m0, %2\n\ts_nop 0\n\tglobal_load_lds_dwordx4 %0, %1 sc1" :: "v"(voff), "s"(sbase), "s"(lds_base) : "memory");
}
__device__ __forceinline__ void glds16m(const void* sbase, u32 voff, u32 lds_base, u64 mask) {
  asm volatile("s_mov_b32 m0, %2\n\ts_mov_b64 exec, %3\n\tglobal_load_lds_dwordx4 %0, %1\n\ts_mov_b64 exec, -1"
               :: "v"(voff), "s"(sbase), "s"(lds_base), "s"(mask) : "memory");
}
__device__ __forceinline__ bf16x8 trfrag(lds_ptr p0, lds_ptr p1) {
  typedef short v4i16_t __attribute__((ext_vector_type(4)));
  typedef short v8i16_t __attribute__((ext_vector_type(8)));
  v4i16_t a = __builtin_amdgcn_ds_read_tr16_b64_v4i16((LAS v4i16_t*)p0);
  v4i16_t b = __builtin_amdgcn_ds_read_tr16_b64_v4i16((LAS v4i16_t*)p1);
  v8i16_t r = __builtin_shufflevector(a, b, 0, 1, 2, 3, 4, 5, 6, 7);
  return __builtin_bit_cast(bf16x8, r);
}
__device__ __forceinline__ u32x4 lds_ld16(lds_ptr p) { return *(LAS u32x4*)p; }
__device__ __forceinline__ f32x4 lds_ldf(lds_ptr p) { return *(LAS f32x4*)p; }
__device__ __forceinline__ void lds_st16(lds_ptr p, u32x4 v) { *(LAS u32x4*)p = v; }
__device__ __forceinline__ void lds_stf(lds_ptr p, f32x4 v) { *(LAS f32x4*)p = v; }
__device__ __forceinline__ u32x4 pair16(f32x4 a, f32x4 b) {
  u32 a0 = cvtpk(a[0], a[1]), a1 = cvtpk(a[2], a[3]), b0 = cvtpk(b[0], b[1]), b1 = cvtpk(b[2], b[3]);
  auto s0 = __builtin_amdgcn_permlane16_swap(a0, b0, false, false);
  auto s1 = __builtin_amdgcn_permlane16_swap(a1, b1, false, false);
  return u32x4{s0[0], s1[0], s0[1], s1[1]};
}
__device__ __forceinline__ u32x4 pair32(f32x4 a, f32x4 b) {
  u32 a0 = cvtpk(a[0], a[1]), a1 = cvtpk(a[2], a[3]), b0 = cvtpk(b[0], b[1]), b1 = cvtpk(b[2], b[3]);
  auto s0 = __builtin_amdgcn_permlane32_swap(a0, b0, false, false);
  auto s1 = __builtin_amdgcn_permlane32_swap(a1, b1, false, false);
  return u32x4{s0[0], s1[0], s0[1], s1[1]};
}
__device__ __forceinline__ f32x2 gelu2(f32x2 x) {
  constexpr float K0 = -2.0f * 0.7978845608028654f * 1.4426950408889634f, K1 = K0 * 0.044715f;
  const f32x2 z = x * __builtin_elementwise_fma(x * x, f32x2{K1, K1}, f32x2{K0, K0});
  const f32x2 d = f32x2{EX(z[0]), EX(z[1])} + 1.0f;
  return x * f32x2{__builtin_amdgcn_rcpf(d[0]), __builtin_amdgcn_rcpf(d[1])};
}
__device__ __forceinline__ f32x4 gelu4(f32x4 x) {
  const f32x2 a = gelu2(f32x2{x[0], x[1]}), c = gelu2(f32x2{x[2], x[3]});
  return f32x4{a[0], a[1], c[0], c[1]};
}
__device__ __forceinline__ f32x4 unpk4(u32x2 v) { return f32x4{bf2f(v[0]), bfhi(v[0]), bf2f(v[1]), bfhi(v[1])}; }
__device__ __forceinline__ float xhalf_max(float m) {
  auto r = __builtin_amdgcn_permlane32_swap(__float_as_uint(m), __float_as_uint(m), false, false);
  return __builtin_fmaxf(__uint_as_float(r[0]), __uint_as_float(r[1]));
}
__device__ __forceinline__ float max16(const f32x16& s) {
  float a = __builtin_fmaxf(s[0], s[1]), b = __builtin_fmaxf(s[2], s[3]);
#pragma unroll
  for (int r = 4; r < 16; r += 4) { a = __builtin_fmaxf(__builtin_fmaxf(a, s[r]), s[r + 1]); b = __builtin_fmaxf(__builtin_fmaxf(b, s[r + 2]), s[r + 3]); }
  return __builtin_fmaxf(a, b);
}
__device__ __forceinline__ f32x16 splat16(float v) { return f32x16{v, v, v, v, v, v, v, v, v, v, v, v, v, v, v, v}; }
template <int CTRL> __device__ __forceinline__ float dppadd(float s) {
  return s + __uint_as_float(__builtin_amdgcn_update_dpp(0u, __float_as_uint(s), CTRL, 0xf, 0xf, true));
}
__device__ __forceinline__ float wave_sum(float s) {
  s = dppadd<0x128>(s); s = dppadd<0x124>(s); s = dppadd<0x122>(s); s = dppadd<0x121>(s);
  auto a = __builtin_amdgcn_permlane16_swap(__float_as_uint(s), __float_as_uint(s), false, false);
  s = __uint_as_float(a[0]) + __uint_as_float(a[1]);
  auto c = __builtin_amdgcn_permlane32_swap(__float_as_uint(s), __float_as_uint(s), false, false);
  return __uint_as_float(c[0]) + __uint_as_float(c[1]);
}
__device__ __forceinline__ u32x4 pack8(const f32x16& s, int r) {
  return u32x4{cvtpk(s[r], s[r + 1]), cvtpk(s[r + 2], s[r + 3]), cvtpk(s[r + 4], s[r + 5]), cvtpk(s[r + 6], s[r + 7])};
}

__global__ void __launch_bounds__(512, 2)
fused_kernel(const u16* __restrict__ x, const u16* __restrict__ wqkv, const u16* __restrict__ wo, const u16* __restrict__ w1,
             const u16* __restrict__ b1, const u16* __restrict__ w2, const u16* __restrict__ b2, const u16* __restrict__ gw,
             u16* __restrict__ out, char* ws) {
  __shared__ __attribute__((aligned(16))) char smem[LDS_BYTES];
  const lds_ptr lds = (lds_ptr)smem;
  const u32 lds0 = (u32)(uintptr_t)lds;
  const int tid = threadIdx.x, lane = tid & 63, w = __builtin_amdgcn_readfirstlane(tid >> 6);
  const int b = blockIdx.x, xc = b & 7, bi = b >> 3;
  const int r16 = lane & 15, g4 = lane >> 4, r32 = lane & 31, hh = lane >> 5;
  const __amdgpu_buffer_rsrc_t rs = __builtin_amdgcn_make_buffer_rsrc(ws, 0, WS_END, 0x00020000);
  const int rb = (xc & 1) * 8 + (bi & 7), cb = (xc >> 1) * 4 + (bi >> 3);
  const int head = xc * 4 + (bi >> 3), qb = bi & 7;
  const int tb = (xc & 3) * 8 + (bi & 7), nb = (xc >> 2) * 4 + (bi >> 3);
  bool alive = true;
  STAMP(0);

  const int rg = w & 3, ch = w >> 2, rot = (rb + 2 * cb) & 7;
  const u32 vx = (u32)(((lane >> 3) * DM + ((lane & 7) ^ (lane >> 3)) * 8) * 2);
  const int l12 = (lane * 43) >> 9, c12 = (lane - l12 * 12) ^ ((w & 1) << 1), l14 = (lane * 37) >> 9;
  const u32 vq = (u32)((l12 * 1536 + (c12 < 8 ? cb * 64 + c12 * 8 : 1024 - 64 + cb * 32 + c12 * 8)) * 2);
  const u32 v1 = (u32)((l14 * FF + (lane - l14 * 14) * 8) * 2);
  const char* xs = (const char*)x + (rb * 128 + 16 * w) * (DM * 2);
  const char* qs = (const char*)wqkv + (8 * w) * (1536 * 2);
  const char* fs = (const char*)w1 + (((w >> 1) * 16 + (w & 1) * 4) * FF + cb * 112) * 2;
#define ISSUE_A(s) do { const int kc = ((s) + rot) & 7; \
    if ((s) < 8) { \
      glds16(xs + kc * 128, vx, lds0 + L_X + ((s) & 3) * 16384 + w * 2048); \
      glds16(xs + kc * 128 + 8 * DM * 2, vx, lds0 + L_X + ((s) & 3) * 16384 + w * 2048 + 1024); \
    } else if ((s) < 16) { \
      glds16m(fs + kc * (64 * FF * 2), v1, lds0 + L_W + ((s) & 3) * WSLOT + w * (8 * 224), 0x00FFFFFFFFFFFFFFull); \
    } } while (0)
#define ISSUE_B(s) do { const int kc = ((s) + rot) & 7; \
    if ((s) < 8) { \
      glds16m(qs + kc * (64 * 1536 * 2), vq, lds0 + L_W + ((s) & 3) * WSLOT + w * (8 * 192), 0x0000FFFFFFFFFFFFull); \
      glds16m(qs + kc * (64 * 1536 * 2) + 4 * 1536 * 2, vq, lds0 + L_W + ((s) & 3) * WSLOT + w * (8 * 192) + 4 * 192, 0x0000FFFFFFFFFFFFull); \
    } else if ((s) < 16) { \
      glds16m(fs + kc * (64 * FF * 2) + 8 * FF * 2, v1, lds0 + L_W + ((s) & 3) * WSLOT + w * (8 * 224) + 4 * 224, 0x00FFFFFFFFFFFFFFull); \
    } } while (0)
  ISSUE_A(0); ISSUE_B(0); STAMP(1); ISSUE_A(1); ISSUE_B(1); ISSUE_A(2); ISSUE_B(2);
  u32x2 epoch;
  asm volatile("global_load_dwordx2 %0, %1, %2 sc1" : "=v"(epoch) : "v"(WS_EPOCH), "s"(ws) : "memory");
#define NW0 9
  STAMP(13);
  u32x4 xf[2][16];
  f32x4 aq[3][2];
  bf16x8 wa[4], wb[4];
#pragma unroll
  for (int t = 0; t < 3; ++t) { aq[t][0] = f32x4{}; aq[t][1] = f32x4{}; }
  const lds_ptr xr0 = lds + L_X + (rg * 32 + r16) * 128 + ((g4 ^ (r16 & 7)) * 16), xr1 = lds + L_X + (rg * 32 + r16) * 128 + (((4 + g4) ^ (r16 & 7)) * 16);
  const lds_ptr wqb = lds + L_W + (8 * g4 + (r16 >> 2)) * 192 + (r16 & 3) * 8;
  const lds_ptr wq0 = wqb + ((2 * ch) ^ (g4 & 1)) * 32, wq1 = wqb + ((2 * ch + 1) ^ (g4 & 1)) * 32, wq2 = wqb + ((4 + ch) ^ (g4 & 1)) * 32;
#define QRD(F, s, ks) do { const int o = ((s) & 3) * WSLOT + (ks) * (32 * 192); \
    F[0] = trfrag(wq0 + o, wq0 + o + 4 * 192); F[1] = trfrag(wq1 + o, wq1 + o + 4 * 192); F[2] = trfrag(wq2 + o, wq2 + o + 4 * 192); } while (0)
#define QMM(F, kk, rt) do { const bf16x8 xv = asbf(xf[rt][kk]); \
    aq[0][rt] = MFMA16(F[0], xv, aq[0][rt]); aq[1][rt] = MFMA16(F[1], xv, aq[1][rt]); aq[2][rt] = MFMA16(xv, F[2], aq[2][rt]); \
    KEEP(aq[0][rt]); KEEP(aq[1][rt]); KEEP(aq[2][rt]); SBAR(); } while (0)
#define QSTAGE(s, n) do { WAITB(n); STAMP(16 + (s)); \
    xf[0][2 * (s)] = lds_ld16(xr0 + ((s) & 3) * 16384); xf[1][2 * (s)] = lds_ld16(xr0 + ((s) & 3) * 16384 + 2048); QRD(wa, s, 0); SBAR(); \
    if ((s) > 0) QMM(wb, 2 * (s) - 1, 0); \
    ISSUE_A((s) + 3); SBAR(); \
    if ((s) > 0) QMM(wb, 2 * (s) - 1, 1); \
    xf[0][2 * (s) + 1] = lds_ld16(xr1 + ((s) & 3) * 16384); xf[1][2 * (s) + 1] = lds_ld16(xr1 + ((s) & 3) * 16384 + 2048); QRD(wb, s, 1); SBAR(); \
    QMM(wa, 2 * (s), 0); \
    ISSUE_B((s) + 3); SBAR(); \
    QMM(wa, 2 * (s), 1); } while (0)
  QSTAGE(0, NW0); QSTAGE(1, NW0); QSTAGE(2, NW0); QSTAGE(3, 8);
  PIN(epoch);
  u64 tag = (((u64)__builtin_amdgcn_readfirstlane(epoch[1]) << 32) | (u64)__builtin_amdgcn_readfirstlane(epoch[0])) + 1;
  asm volatile("" : "+s"(tag));
  QSTAGE(4, 8); QSTAGE(5, 8); QSTAGE(6, 6); QSTAGE(7, 4);
  QMM(wb, 15, 0); QMM(wb, 15, 1);
  STAMP(2);
  {
    const int tile = rb * 4 + rg;
    const float qsc = cb < 8 ? QSCALE : 1.0f;
    const u32 qkb = (cb < 8 ? WS_Q + (u32)(((cb * 4 + 2 * ch) * 64 + tile) * 1024) : WS_KV + (u32)(((cb - 8) * 4 + 2 * ch) * 131072 + tile * 2048)) + lane * 16;
    const u32 hstride = cb < 8 ? 65536u : 131072u;
    ST16(pair16(aq[0][0] * qsc, aq[0][1] * qsc), qkb);
    ST16(pair16(aq[1][0] * qsc, aq[1][1] * qsc), qkb + hstride);
    ST16(pair32(aq[2][0], aq[2][1]), WS_KV + (cb * 2 + ch) * 131072 + tile * 2048 + 1024 + lane * 16);
  }
  STAMP(3);
  f32x4 ah[4][2];
#pragma unroll
  for (int c = 0; c < 4; ++c) { ah[c][0] = f32x4{}; ah[c][1] = f32x4{}; }
  const lds_ptr w1b = lds + L_W + (16 * (g4 >> 1) + 4 * (g4 & 1) + (r16 >> 2)) * 224 + (r16 & 3) * 8 + ch * 128;
#define FRD(F, s, ks) do { const lds_ptr p = w1b + ((s) & 3) * WSLOT + (ks) * (32 * 224); \
    F[0] = trfrag(p, p + 8 * 224); F[1] = trfrag(p + 32, p + 32 + 8 * 224); F[2] = trfrag(p + 64, p + 64 + 8 * 224); \
    if (ch == 0) F[3] = trfrag(p + 96, p + 96 + 8 * 224); } while (0)
#define FMM(F, kk, rt) do { const bf16x8 xv = asbf(xf[rt][kk]); \
    ah[0][rt] = MFMA16(F[0], xv, ah[0][rt]); ah[1][rt] = MFMA16(F[1], xv, ah[1][rt]); ah[2][rt] = MFMA16(F[2], xv, ah[2][rt]); \
    KEEP(ah[0][rt]); KEEP(ah[1][rt]); KEEP(ah[2][rt]); \
    if (ch == 0) { ah[3][rt] = MFMA16(F[3], xv, ah[3][rt]); KEEP(ah[3][rt]); } SBAR(); } while (0)
#define KVDMA(j) do { const int sl_ = ((j) >> 1) * 4 + (w >> 1) + 32 * ((j) & 1); \
    glds16c(ws + WS_KV + head * 131072 + ((sl_ + qb * 8) & 63) * 2048 + (w & 1) * 1024, lane * 16, lds0 + sl_ * KVS + (w & 1) * 1024); } while (0)
#define FSTAGE(s, n, MID, END) do { WAITB(n); STAMP(16 + (s)); \
    FRD(wa, s, 0); SBAR(); \
    if ((s) > 8) FMM(wb, 2 * ((s) - 8) - 1, 0); \
    ISSUE_A((s) + 3); SBAR(); \
    if ((s) > 8) FMM(wb, 2 * ((s) - 8) - 1, 1); \
    FRD(wb, s, 1); SBAR(); \
    FMM(wa, 2 * ((s) - 8), 0); \
    ISSUE_B((s) + 3); MID; SBAR(); \
    FMM(wa, 2 * ((s) - 8), 1); \
    END; } while (0)
  FSTAGE(8, 7, , ); FSTAGE(9, 7, , );
  FSTAGE(10, 4, , );
  if (tid == 0) __hip_atomic_store((GAS u64*)(ws + WS_FLAG1 + b * 8), tag, RLX_AGENT);
  STAMP(4);
  FSTAGE(11, 4, , );
  const int grow = rb * 128 + cb * 8 + w;
  u32x4 gx = *(const u32x4*)(x + grow * DM + lane * 8), gv = *(const u32x4*)(gw + lane * 8);
  u32x2 b1v[4];
#pragma unroll
  for (int c = 0; c < 4; ++c) b1v[c] = *(const u32x2*)(b1 + cb * 112 + (c == 3 ? 48 : ch * 64 + c * 16) + g4 * 4);
  u32 pt[10];
  {
    const int f = b * 9 + w, fo = f - 1792;
    const bool isw2 = f < 1792;
    const u16* src = isw2 ? w2 + ((f % 112) * 16 + 8 * hh) * DM + (f / 112) * 32 + r32 : wo + ((fo & 31) * 16 + 4 * hh) * DM + (fo >> 5) * 32 + r32;
    const int hi = isw2 ? 4 * DM : 8 * DM;
#pragma unroll
    for (int j = 0; j < 4; ++j) { pt[j] = src[j * DM]; pt[4 + j] = src[hi + j * DM]; }
  }
  {
    const int f = b * 9 + 8, fo = f - 1792;
    const bool isw2 = f < 1792;
    const u16* src = isw2 ? w2 + ((f % 112) * 16 + 8 * hh + 2 * (w & 3)) * DM + (f / 112) * 32 + r32
                          : wo + ((fo & 31) * 16 + 4 * hh + 2 * (w & 1) + 4 * (w & 2)) * DM + (fo >> 5) * 32 + r32;
    pt[8] = src[0]; pt[9] = src[DM];
  }
  const int prb = lane < 32 ? r16 : 2 * qb + (lane & 1), pcb = lane < 16 ? 8 + (head >> 2) : lane < 32 ? (head >> 1) : (head >> 2);
  const u32 f1off = WS_FLAG1 + (((pcb >> 2) << 1 | (prb >> 3)) | (((pcb & 3) << 3 | (prb & 7)) << 3)) * 8;
  const int qg = w & 3, kh = w >> 2;
  u64 f1v;
  u32x4 qAu, qBu;
#define F1LOAD() asm volatile("global_load_dwordx2 %0, %1, %2 sc1" : "=v"(f1v) : "v"(f1off), "s"(ws) : "memory")
#define F1CHECK() do { \
    asm volatile("s_waitcnt vmcnt(0)" : "+v"(f1v) :: "memory"); \
    asm volatile("" : "+v"(gx), "+v"(gv), "+v"(b1v[0]), "+v"(b1v[1]), "+v"(b1v[2]), "+v"(b1v[3]), "+v"(pt[8]), "+v"(pt[9])); \
    asm volatile("" : "+v"(pt[0]), "+v"(pt[1]), "+v"(pt[2]), "+v"(pt[3]), "+v"(pt[4]), "+v"(pt[5]), "+v"(pt[6]), "+v"(pt[7])); \
    for (u32 spins = 0; !__all(f1v == tag);) { \
      if (++spins > SPIN_CAP) { alive = false; break; } \
      __builtin_amdgcn_s_sleep(1); \
      f1v = __hip_atomic_load((GAS u64*)(ws + f1off), RLX_AGENT); } \
    asm volatile("" ::: "memory"); STAMP(6); \
    const u32 qo = WS_Q + (head * 64 + qb * 8 + qg * 2) * 1024 + lane * 16; \
    asm volatile("global_load_dwordx4 %0, %2, %3 sc1\n\tglobal_load_dwordx4 %1, %2, %3 offset:1024 sc1" : "=&v"(qAu), "=&v"(qBu) : "v"(qo), "s"(ws) : "memory"); \
    KVDMA(0); KVDMA(1); } while (0)
  FSTAGE(12, 20, , F1LOAD());
  FSTAGE(13, 21, , F1CHECK());
  FSTAGE(14, 63, KVDMA(2); KVDMA(3), KVDMA(4); KVDMA(5));
  FSTAGE(15, 63, KVDMA(6); KVDMA(7), KVDMA(8); KVDMA(10));
  FMM(wb, 15, 0); FMM(wb, 15, 1);
  STAMP(5);
  BARRIER();
  KVDMA(12); KVDMA(14);
  if ((tid & 7) < 6) { const u32 one = ((tid & 3) == 0) ? 0x3F803F80u : 0u; lds_st16(lds + (tid >> 3) * KVS + 2048 + (tid & 7) * 16, u32x4{one, one, one, one}); }
  {
    const u32 hb = WS_H + (u32)(((rb * 4 + rg) * 112 + cb * 7 + ch * 4) * 1024) + lane * 16;
#pragma unroll
    for (int c = 0; c < 4; ++c) {
      if (c < 3 || ch == 0) {
        const f32x4 bb = unpk4(b1v[c]);
        ST16(pair16(gelu4(ah[c][0] + bb), gelu4(ah[c][1] + bb)), hb + c * 1024);
      }
      if (c == 0) { KVDMA(9); KVDMA(11); }
      if (c == 1) { KVDMA(13); KVDMA(15); }
    }
    float s = 0.f;
#pragma unroll
    for (int e = 0; e < 4; ++e) { const u32 ua = gx[e], ub = gv[e]; s = __builtin_amdgcn_fdot2_f32_bf16(__builtin_bit_cast(bf16x2, ua), __builtin_bit_cast(bf16x2, ub), s, false); }
    const float z = rbf(wave_sum(s)), g = rbf(__builtin_amdgcn_rcpf(1.0f + EX(z * -1.4426950408889634f)));
    if (lane == 0) __builtin_amdgcn_raw_buffer_store_b32(__float_as_uint(g), rs, WS_G + grow * 4, 0, AUX_SC1);
    ST16((u32x4{pt[0] | (pt[1] << 16), pt[2] | (pt[3] << 16), pt[4] | (pt[5] << 16), pt[6] | (pt[7] << 16)}),
         WS_W2F + (b * 9 + w) * 1024 + ((b * 9 + w) < 1792 ? 0 : WS_WOF - WS_W2F - 1792 * 1024) + lane * 16);
    if (w < 4) __builtin_amdgcn_raw_buffer_store_b32(pt[8] | (pt[9] << 16), rs, WS_W2F + (b * 9 + 8) * 1024 + ((b * 9 + 8) < 1792 ? 0 : WS_WOF - WS_W2F - 1792 * 1024) + lane * 16 + w * 4, 0, AUX_SC1);
    ST16((u32x4{~0u, ~0u, ~0u, ~0u}), WS_O + ((qb * 8 + w) * 32 + head) * 1024 + lane * 16);
  }
  STAMP(7);
  asm volatile("s_waitcnt vmcnt(14) lgkmcnt(0)\n\ts_barrier" : "+v"(qAu), "+v"(qBu) :: "memory");
  const bf16x8 qA = asbf(qAu), qB = asbf(qBu);
  STAMP(8);

  lds_ptr kp = lds + kh * (32 * KVS) + lane * 16;
  const bool grp2 = (r32 >= 16 && r32 < 20) || r32 >= 28;
  const int coff = 2048 + (r32 == 16 ? 0 : r32 == 20 ? 64 : grp2 ? 16 : 80);
  lds_ptr vp0 = lds + kh * (32 * KVS) + (r32 < 16 ? 1024 + hh * 256 + r32 * 16 : coff), vp1 = lds + kh * (32 * KVS) + (r32 < 16 ? 1536 + hh * 256 + r32 * 16 : coff);
  f32x16 sA, sB, oA = splat16(0.f), oB = splat16(0.f), cn16;
  f32x16 fa[2][2];
  fa[0][0] = splat16(0.f); fa[0][1] = splat16(0.f); fa[1][0] = splat16(0.f); fa[1][1] = splat16(0.f);
  u32x4 kf, v0, v1f, p1 = u32x4{0, 0, 0, 0};
  u32x4 sh[2][2], sw[2][2];
  {
    kf = lds_ld16(kp); v0 = lds_ld16(vp0); v1f = lds_ld16(vp1);
    sA = MFMA32(asbf(kf), qA, splat16(0.f));
    sB = MFMA32(asbf(kf), qB, splat16(0.f));
    cn16 = splat16(-xhalf_max(__builtin_fmaxf(max16(sA), max16(sB))));
    PIN(cn16);
    sA = MFMA32(asbf(kf), qA, cn16);
  }
  const u32 hoff = WS_H + (u32)((tb * 2 * 112 + w * 14) * 1024) + lane * 16;
  const u32 woff = WS_W2F + (u32)((nb * 2 * 112 + w * 14) * 1024) + lane * 16;
  int fk = (tb & 7) + 4 * (nb & 3); fk = fk >= 14 ? fk - 14 : fk;
#define UNIT(SC, SN, QN, OC, OP, RD0, RD1, RD2, FMX, LD) do { \
    SN = MFMA32(asbf(kf), QN, cn16); KEEP(SN); RD0; \
    SBAR(); \
    SC[0] = EX(SC[0]); SC[1] = EX(SC[1]); SC[2] = EX(SC[2]); SC[3] = EX(SC[3]); PIN(SC); \
    SBAR(); \
    OP = MFMA32(asbf(v1f), asbf(p1), OP); KEEP(OP); RD1; \
    SBAR(); \
    u32x4 p0; \
    SC[4] = EX(SC[4]); SC[5] = EX(SC[5]); SC[6] = EX(SC[6]); SC[7] = EX(SC[7]); p0[0] = cvtpk(SC[0], SC[1]); p0[1] = cvtpk(SC[2], SC[3]); PIN(SC); PIN(p0); \
    SBAR(); \
    FMX; LD; \
    SBAR(); \
    p0[2] = cvtpk(SC[4], SC[5]); p0[3] = cvtpk(SC[6], SC[7]); PIN(p0); SBAR(); \
    SC[8] = EX(SC[8]); SC[9] = EX(SC[9]); SC[10] = EX(SC[10]); PIN(SC); \
    SBAR(); \
    OC = MFMA32(asbf(v0), asbf(p0), OC); KEEP(OC); RD2; \
    SBAR(); \
    SC[11] = EX(SC[11]); SC[12] = EX(SC[12]); SC[13] = EX(SC[13]); SC[14] = EX(SC[14]); SC[15] = EX(SC[15]); \
    p1 = pack8(SC, 8); PIN(p1); \
    SBAR(); } while (0)
#define UA(t, FMX, LD) UNIT(sA, sB, qB, oA, oB, kf = lds_ld16(kp + ((t) + 1) * KVS), v1f = lds_ld16(vp1 + (t) * KVS), , FMX, LD)
#define UB(t, FMX, LD) UNIT(sB, sA, qA, oB, oA, , , v0 = lds_ld16(vp0 + ((t) + 1) * KVS), FMX, LD)
#define ADV() do { kp += 4 * KVS; vp0 += 4 * KVS; vp1 += 4 * KVS; } while (0)
#define LDH(s, i, so) sh[s][i] = LD16(hoff, (so) + (i) * (112 * 1024))
#define LDW(s, i, so) sw[s][i] = LD16(woff, (so) + (i) * (112 * 1024))
#define FM(s, n, t) fa[n][t] = MFMA32(asbf(sw[s][n]), asbf(sh[s][t]), fa[n][t]); KEEP(fa[n][t])
#define NEXTK() __extension__({ const int o_ = fk * 1024; fk = fk == 13 ? 0 : fk + 1; o_; })
  UA(0, , ); UB(0, , ); UA(1, , ); UB(1, , );
  WAITB(0);
  if (tid == 0) __hip_atomic_store((GAS u64*)(ws + WS_FLAG2 + b * 8), tag, RLX_AGENT);
  UA(2, , ); UB(2, , ); UA(3, , ); UB(3, , ); ADV();
  u32x4 f0, f1;
  UA(0, , ); UB(0, , f0 = LD16(WS_FLAG2 + lane * 32, 0); f1 = LD16(WS_FLAG2 + lane * 32 + 16, 0)); UA(1, , ); UB(1, , );
  for (u32 spins = 0; alive;) {
    const u32 lo = (u32)tag, hi = (u32)(tag >> 32);
    const bool ok = f0[0] == lo && f0[1] == hi && f0[2] == lo && f0[3] == hi && f1[0] == lo && f1[1] == hi && f1[2] == lo && f1[3] == hi;
    if (__all(ok)) break;
    if (++spins > SPIN_CAP) { alive = false; break; }
    __builtin_amdgcn_s_sleep(1);
    f0 = LD16(WS_FLAG2 + lane * 32, 0); f1 = LD16(WS_FLAG2 + lane * 32 + 16, 0);
  }
  asm volatile("" ::: "memory");
  if (b == 0 && tid == 0 && alive) __hip_atomic_store((GAS u64*)(ws + WS_EPOCH), tag, RLX_AGENT);
  STAMP(9);
  { const int s0 = NEXTK(), s1 = NEXTK();
    LDH(0, 0, s0); LDW(0, 0, s0); LDW(0, 1, s0); LDH(0, 1, s0); LDH(1, 0, s1); LDW(1, 0, s1); LDW(1, 1, s1); LDH(1, 1, s1); }
  const int tb2 = w & 1, kq = w >> 1;
  const float gq = __uint_as_float(__builtin_amdgcn_raw_buffer_load_b32(rs, WS_G + (qb * 256 + qg * 64 + kh * 32 + r32) * 4, 0, AUX_SC1));
  const float gt = __uint_as_float(__builtin_amdgcn_raw_buffer_load_b32(rs, WS_G + (tb * 64 + tb2 * 32 + r32) * 4, 0, AUX_SC1));
  UA(2, , ); UB(2, , ); UA(3, , ); UB(3, , ); ADV();
  for (int it = 2; it < 8; ++it) {
    const int s0 = NEXTK(), s1 = NEXTK();
    UA(0, FM(0, 0, 0), ); UB(0, FM(0, 1, 0), LDH(0, 0, s0)); UA(1, FM(0, 0, 1), LDW(0, 0, s0)); UB(1, FM(0, 1, 1), LDW(0, 1, s0); LDH(0, 1, s0));
    UA(2, FM(1, 0, 0), ); UB(2, FM(1, 1, 0), LDH(1, 0, s1)); UA(3, FM(1, 0, 1), LDW(1, 0, s1)); UB(3, FM(1, 1, 1), LDW(1, 1, s1); LDH(1, 1, s1));
    ADV();
  }
  oB = MFMA32(asbf(v1f), asbf(p1), oB);
  STAMP(10);
  float cA = cn16[0], cB = cA;
  if (__builtin_expect(!__all(oA[8] <= 0x1p30f && oB[8] <= 0x1p30f && oA[8] >= 0x1p-60f && oB[8] >= 0x1p-60f), 0)) {
    const lds_ptr k2 = kp - 32 * KVS, v2 = vp0 - 32 * KVS, v3 = vp1 - 32 * KVS;
#define REDO(Q, O, C) do { float m = -INFINITY; \
      for (int t = 0; t < 32; ++t) m = __builtin_fmaxf(m, max16(MFMA32(asbf(lds_ld16(k2 + t * KVS)), Q, splat16(0.f)))); \
      C = -xhalf_max(m); cn16 = splat16(C); O = splat16(0.f); \
      for (int t = 0; t < 32; ++t) { \
        f32x16 s = MFMA32(asbf(lds_ld16(k2 + t * KVS)), Q, cn16); \
        _Pragma("unroll") for (int r = 0; r < 16; ++r) s[r] = EX(s[r]); \
        O = MFMA32(asbf(lds_ld16(v2 + t * KVS)), asbf(pack8(s, 0)), O); O = MFMA32(asbf(lds_ld16(v3 + t * KVS)), asbf(pack8(s, 8)), O); } } while (0)
    REDO(qA, oA, cA); REDO(qB, oB, cB);
  }
  const int ncol = nb * 64 + kq * 16;
  const u32x2 bia = *(const u32x2*)(b2 + ncol + 4 * hh), bib = *(const u32x2*)(b2 + ncol + 8 + 4 * hh);
  {
    const f32x16 os = kh ? oA : oB;
    const float cs = kh ? cA : cB;
    const lds_ptr p = lds + L_CMB + w * 2560 + lane * 16;
    lds_stf(p, f32x4{os[0], os[1], os[2], os[3]}); lds_stf(p + 1024, f32x4{os[4], os[5], os[6], os[7]});
    *(LAS f32x2*)(lds + L_CMB + w * 2560 + 2048 + lane * 8) = f32x2{os[8], cs};
  }
  BARRIER();
  {
    const f32x16 om = kh ? oB : oA;
    const float cm = kh ? cB : cA;
    const lds_ptr p = lds + L_CMB + (w ^ 4) * 2560 + lane * 16;
    const f32x4 a = lds_ldf(p), c = lds_ldf(p + 1024);
    const f32x2 lc = *(LAS f32x2*)(lds + L_CMB + (w ^ 4) * 2560 + 2048 + lane * 8);
    const float cn = __builtin_fminf(cm, lc[1]);
    float e0 = EX(cn - cm), e1 = EX(cn - lc[1]);
    const float sc = gq * __builtin_amdgcn_rcpf(om[8] * e0 + lc[0] * e1);
    e0 *= sc; e1 *= sc;
    u32x4 o = u32x4{cvtpk(om[0] * e0 + a[0] * e1, om[1] * e0 + a[1] * e1), cvtpk(om[2] * e0 + a[2] * e1, om[3] * e0 + a[3] * e1),
                    cvtpk(om[4] * e0 + c[0] * e1, om[5] * e0 + c[1] * e1), cvtpk(om[6] * e0 + c[2] * e1, om[7] * e0 + c[3] * e1)};
#pragma unroll
    for (int e = 0; e < 4; ++e) o[e] = (o[e] == ~0u) ? 0x7FC07FC0u : o[e];
    ST16(o, WS_O + ((qb * 8 + qg * 2 + kh) * 32 + head) * 1024 + lane * 16);
  }
  STAMP(14);
  SBAR();
  u32x4 wf[8][2], of[8];
#define OLOAD() _Pragma("unroll") for (int j = 0; j < 8; ++j) of[j] = LD16(WS_O + ((tb * 2 + tb2) * 32 + kq * 8 + j) * 1024 + lane * 16, 0)
#pragma unroll
  for (int j = 0; j < 8; ++j) {
    wf[j][0] = LD16(WS_WOF + ((nb * 2) * 32 + kq * 8 + j) * 1024 + lane * 16, 0);
    wf[j][1] = LD16(WS_WOF + ((nb * 2 + 1) * 32 + kq * 8 + j) * 1024 + lane * 16, 0);
  }
  FM(0, 0, 0); FM(0, 1, 0); FM(0, 0, 1); FM(0, 1, 1); FM(1, 0, 0); FM(1, 1, 0); FM(1, 0, 1); FM(1, 1, 1);
#define Q4(a, r) f32x4{(a)[r], (a)[(r) + 1], (a)[(r) + 2], (a)[(r) + 3]}
#pragma unroll
  for (int d = 0; d < 8; ++d) {
    const lds_ptr p = lds + (d * 8 + w) * 2048 + lane * 16;
    lds_stf(p, Q4(fa[d >> 2][d & 1], ((d >> 1) & 1) * 8));
    lds_stf(p + 1024, Q4(fa[d >> 2][d & 1], ((d >> 1) & 1) * 8 + 4));
  }
  OLOAD();
  BARRIER();
  f32x4 lo = unpk4(bia), hi = unpk4(bib);
#pragma unroll
  for (int s = 0; s < 8; ++s) {
    const lds_ptr p = lds + (w * 8 + s) * 2048 + lane * 16;
    lo += lds_ldf(p); hi += lds_ldf(p + 1024);
  }
  { const float og = rbf(1.0f - gt); lo *= og; hi *= og; }
  PIN(lo); PIN(hi);
  BARRIER();
  for (u32 spins = 0;;) {
    bool ok = true;
#pragma unroll
    for (int j = 0; j < 8; ++j) ok = ok && of[j][0] != ~0u && of[j][1] != ~0u && of[j][2] != ~0u && of[j][3] != ~0u;
    if (__all(ok) || !alive) break;
    if (++spins > SPIN_CAP) { alive = false; break; }
    asm volatile("" ::: "memory");
    OLOAD();
  }
  STAMP(11);
  f32x16 pa[2] = {splat16(0.f), splat16(0.f)};
#pragma unroll
  for (int j = 0; j < 8; ++j) { pa[0] = MFMA32(asbf(wf[j][0]), asbf(of[j]), pa[0]); pa[1] = MFMA32(asbf(wf[j][1]), asbf(of[j]), pa[1]); }
#pragma unroll
  for (int q = 0; q < 4; ++q) {
    const lds_ptr p = lds + ((q * 2 + tb2) * 4 + kq) * 2048 + lane * 16;
    lds_stf(p, Q4(pa[q >> 1], (q & 1) * 8));
    lds_stf(p + 1024, Q4(pa[q >> 1], (q & 1) * 8 + 4));
  }
  BARRIER();
#pragma unroll
  for (int s = 0; s < 4; ++s) {
    const lds_ptr p = lds + (w * 4 + s) * 2048 + lane * 16;
    lo += lds_ldf(p); hi += lds_ldf(p + 1024);
  }
  *(u32x4*)(out + (tb * 64 + tb2 * 32 + r32) * DM + ncol + 8 * hh) = pair32(lo, hi);
  STAMP(12);
}

extern "C" void kernel_launch(void* const* d_in, const int* in_sizes, int n_in, void* d_out, int out_size, void* d_ws, size_t ws_size,
                              hipStream_t stream) {
  const u16 *x = (const u16*)d_in[0], *wqkv = (const u16*)d_in[1], *wo = (const u16*)d_in[2], *w1 = (const u16*)d_in[4],
            *b1 = (const u16*)d_in[5], *w2 = (const u16*)d_in[6], *b2 = (const u16*)d_in[7], *gw = (const u16*)d_in[8];
  fused_kernel<<<256, 512, 0, stream>>>(x, wqkv, wo, w1, b1, w2, b2, gw, (u16*)d_out, (char*)d_ws);
}
```

```cpp
#include <hip/hip_runtime.h>
#include <hip/hip_bf16.h>
#include <stdint.h>

typedef unsigned short u16;
typedef unsigned int u32;
typedef unsigned long long u64;
typedef __attribute__((ext_vector_type(8))) __bf16 bf16x8;
typedef __attribute__((ext_vector_type(2))) __bf16 bf16x2;
typedef __attribute__((ext_vector_type(4))) float f32x4;
typedef __attribute__((ext_vector_type(2))) float f32x2;
typedef __attribute__((ext_vector_type(16))) float f32x16;
typedef __attribute__((ext_vector_type(4))) u32 u32x4;
typedef __attribute__((ext_vector_type(2))) u32 u32x2;
#define LAS __attribute__((address_space(3)))
#define GAS __attribute__((address_space(1)))
typedef LAS char* lds_ptr;

constexpr int DM = 512, FF = 1792;
constexpr u32 WS_EPOCH = 0, WS_FLAG1 = 4096, WS_FLAG2 = 8192, WS_G = 16384, WS_STAMP = 20u << 20;
constexpr u32 WS_Q = 1u << 20, WS_KV = 3u << 20, WS_H = 7u << 20, WS_W2F = 14u << 20, WS_WOF = 16u << 20, WS_O = 17u << 20, WS_END = 19u << 20;
constexpr int L_X = 0, L_W = 104 * 1024, WSLOT = 14336, KVS = 2176, L_CMB = 64 * KVS, L_PT = L_W, L_R = 16 * KVS, LDS_BYTES = 160 * 1024;
constexpr int AUX_SC1 = 16;
constexpr int CODE_LINES = 160;
constexpr u32 SPIN_CAP = 1u << 15;
constexpr float QSCALE = 0.25f * 1.4426950408889634f;

#define SBAR() __builtin_amdgcn_sched_barrier(0)
#define PIN(x) asm volatile("" : "+v"(x))
#define KEEP(x) asm volatile("" :: "v"(x))
#define MFMA32(a, b, c) __builtin_amdgcn_mfma_f32_32x32x16_bf16(a, b, c, 0, 0, 0)
#define MFMA16(a, b, c) __builtin_amdgcn_mfma_f32_16x16x32_bf16(a, b, c, 0, 0, 0)
#define BARRIER() asm volatile("s_waitcnt lgkmcnt(0)\n\ts_barrier" ::: "memory")
#define WAITB_(n) asm volatile("s_waitcnt vmcnt(" #n ") lgkmcnt(0)\n\ts_barrier" ::: "memory")
#define WAITB(n) WAITB_(n)
#define RLX_AGENT __ATOMIC_RELAXED, __HIP_MEMORY_SCOPE_AGENT
#define EX(v) __builtin_amdgcn_exp2f(v)
#define LD16(off, soff) __builtin_amdgcn_raw_buffer_load_b128(rs, off, soff, AUX_SC1)
#define ST16(v, off) __builtin_amdgcn_raw_buffer_store_b128(v, rs, off, 0, AUX_SC1)

#define STAMP(i) do { } while (0)

__device__ __forceinline__ float bf2f(u32 v) { return __uint_as_float(v << 16); }
__device__ __forceinline__ float bfhi(u32 v) { return __uint_as_float(v & 0xffff0000u); }
__device__ __forceinline__ u32 cvtpk(float lo, float hi) { bf16x2 r = {(__bf16)lo, (__bf16)hi}; return __builtin_bit_cast(u32, r); }
__device__ __forceinline__ float rbf(float f) { return bf2f(cvtpk(f, f)); }
__device__ __forceinline__ bf16x8 asbf(u32x4 v) { return __builtin_bit_cast(bf16x8, v); }
typedef __amdgpu_buffer_rsrc_t rsrc_t;
__device__ __forceinline__ void dma16(rsrc_t r, u32 voff, u32 soff, u32 lds_base) {
  asm volatile("s_mov_b32 m0, %3\n\ts_nop 0\n\tbuffer_load_dwordx4 %0, %1, %2 offen lds" :: "v"(voff), "s"(r), "s"(soff), "s"(lds_base) : "memory");
}
__device__ __forceinline__ void dma16c(rsrc_t r, u32 voff, u32 soff, u32 lds_base) {
  asm volatile("s_mov_b32 m0, %3\n\ts_nop 0\n\tbuffer_load_dwordx4 %0, %1, %2 offen sc1 lds" :: "v"(voff), "s"(r), "s"(soff), "s"(lds_base) : "memory");
}
__device__ __forceinline__ void dma16m(rsrc_t r, u32 voff, u32 soff, u32 lds_base, u64 mask) {
  asm volatile("s_mov_b32 m0, %3\n\ts_mov_b64 exec, %4\n\tbuffer_load_dwordx4 %0, %1, %2 offen lds\n\ts_mov_b64 exec, -1"
               :: "v"(voff), "s"(r), "s"(soff), "s"(lds_base), "s"(mask) : "memory");
}
__device__ __forceinline__ bf16x8 trfrag(lds_ptr p0, lds_ptr p1) {
  typedef short v4i16_t __attribute__((ext_vector_type(4)));
  typedef short v8i16_t __attribute__((ext_vector_type(8)));
  v4i16_t a = __builtin_amdgcn_ds_read_tr16_b64_v4i16((LAS v4i16_t*)p0);
  v4i16_t b = __builtin_amdgcn_ds_read_tr16_b64_v4i16((LAS v4i16_t*)p1);
  v8i16_t r = __builtin_shufflevector(a, b, 0, 1, 2, 3, 4, 5, 6, 7);
  return __builtin_bit_cast(bf16x8, r);
}
__device__ __forceinline__ u32x4 lds_ld16(lds_ptr p) { return *(LAS u32x4*)p; }
__device__ __forceinline__ f32x4 lds_ldf(lds_ptr p) { return *(LAS f32x4*)p; }
__device__ __forceinline__ void lds_st16(lds_ptr p, u32x4 v) { *(LAS u32x4*)p = v; }
__device__ __forceinline__ void lds_stf(lds_ptr p, f32x4 v) { *(LAS f32x4*)p = v; }
__device__ __forceinline__ u32x4 pair16(f32x4 a, f32x4 b) {
  u32 a0 = cvtpk(a[0], a[1]), a1 = cvtpk(a[2], a[3]), b0 = cvtpk(b[0], b[1]), b1 = cvtpk(b[2], b[3]);
  auto s0 = __builtin_amdgcn_permlane16_swap(a0, b0, false, false);
  auto s1 = __builtin_amdgcn_permlane16_swap(a1, b1, false, false);
  return u32x4{s0[0], s1[0], s0[1], s1[1]};
}
__device__ __forceinline__ u32x4 pair32(f32x4 a, f32x4 b) {
  u32 a0 = cvtpk(a[0], a[1]), a1 = cvtpk(a[2], a[3]), b0 = cvtpk(b[0], b[1]), b1 = cvtpk(b[2], b[3]);
  auto s0 = __builtin_amdgcn_permlane32_swap(a0, b0, false, false);
  auto s1 = __builtin_amdgcn_permlane32_swap(a1, b1, false, false);
  return u32x4{s0[0], s1[0], s0[1], s1[1]};
}
__device__ __forceinline__ f32x2 gelu2(f32x2 x) {
  constexpr float K0 = -2.0f * 0.7978845608028654f * 1.4426950408889634f, K1 = K0 * 0.044715f;
  const f32x2 z = x * __builtin_elementwise_fma(x * x, f32x2{K1, K1}, f32x2{K0, K0});
  const f32x2 d = f32x2{EX(z[0]), EX(z[1])} + 1.0f;
  return x * f32x2{__builtin_amdgcn_rcpf(d[0]), __builtin_amdgcn_rcpf(d[1])};
}
__device__ __forceinline__ f32x4 gelu4(f32x4 x) {
  const f32x2 a = gelu2(f32x2{x[0], x[1]}), c = gelu2(f32x2{x[2], x[3]});
  return f32x4{a[0], a[1], c[0], c[1]};
}
__device__ __forceinline__ f32x4 unpk4(u32x2 v) { return f32x4{bf2f(v[0]), bfhi(v[0]), bf2f(v[1]), bfhi(v[1])}; }
__device__ __forceinline__ float xhalf_max(float m) {
  auto r = __builtin_amdgcn_permlane32_swap(__float_as_uint(m), __float_as_uint(m), false, false);
  return __builtin_fmaxf(__uint_as_float(r[0]), __uint_as_float(r[1]));
}
__device__ __forceinline__ float max16(const f32x16& s) {
  float a = __builtin_fmaxf(s[0], s[1]), b = __builtin_fmaxf(s[2], s[3]);
#pragma unroll
  for (int r = 4; r < 16; r += 4) { a = __builtin_fmaxf(__builtin_fmaxf(a, s[r]), s[r + 1]); b = __builtin_fmaxf(__builtin_fmaxf(b, s[r + 2]), s[r + 3]); }
  return __builtin_fmaxf(a, b);
}
__device__ __forceinline__ f32x16 splat16(float v) { return f32x16{v, v, v, v, v, v, v, v, v, v, v, v, v, v, v, v}; }
template <int CTRL> __device__ __forceinline__ float dppadd(float s) {
  return s + __uint_as_float(__builtin_amdgcn_update_dpp(0u, __float_as_uint(s), CTRL, 0xf, 0xf, true));
}
__device__ __forceinline__ float wave_sum(float s) {
  s = dppadd<0x128>(s); s = dppadd<0x124>(s); s = dppadd<0x122>(s); s = dppadd<0x121>(s);
  auto a = __builtin_amdgcn_permlane16_swap(__float_as_uint(s), __float_as_uint(s), false, false);
  s = __uint_as_float(a[0]) + __uint_as_float(a[1]);
  auto c = __builtin_amdgcn_permlane32_swap(__float_as_uint(s), __float_as_uint(s), false, false);
  return __uint_as_float(c[0]) + __uint_as_float(c[1]);
}
__device__ __forceinline__ u32x4 pack8(const f32x16& s, int r) {
  return u32x4{cvtpk(s[r], s[r + 1]), cvtpk(s[r + 2], s[r + 3]), cvtpk(s[r + 4], s[r + 5]), cvtpk(s[r + 6], s[r + 7])};
}

__global__ void __launch_bounds__(512, 2)
fused_kernel(const u16* __restrict__ x, const u16* __restrict__ wqkv, const u16* __restrict__ wo, const u16* __restrict__ w1,
             const u16* __restrict__ b1, const u16* __restrict__ w2, const u16* __restrict__ b2, const u16* __restrict__ gw,
             u16* __restrict__ out, char* ws) {
  __shared__ __attribute__((aligned(16))) char smem[LDS_BYTES];
  const lds_ptr lds = (lds_ptr)smem;
  const u32 lds0 = (u32)(uintptr_t)lds;
  const int tid = threadIdx.x, lane = tid & 63, w = __builtin_amdgcn_readfirstlane(tid >> 6);
  const int b = blockIdx.x, xc = b & 7, bi = b >> 3;
  u32 cpf;
  {
    u64 pc; asm volatile("s_getpc_b64 %0" : "=s"(pc));
    const int ln = bi * 8 + w;
    asm volatile("global_load_dword %0, %1, %2" : "=v"(cpf) : "v"((u32)((ln < CODE_LINES ? ln : 0) * 128 + (lane & 31) * 4)), "s"(pc & ~127ull) : "memory");
  }
  const int r16 = lane & 15, g4 = lane >> 4, r32 = lane & 31, hh = lane >> 5;
  const rsrc_t rs = __builtin_amdgcn_make_buffer_rsrc(ws, 0, WS_END, 0x00020000);
  const int rb = (xc & 1) * 8 + (bi & 7), cb = (xc >> 1) * 4 + (bi >> 3);
  const int head = xc * 4 + (bi >> 3), qb = bi & 7;
  const int tb = (xc & 3) * 8 + (bi & 7), nb = (xc >> 2) * 4 + (bi >> 3);
  bool alive = true;
  STAMP(0);

  const int rg = w & 3, ch = w >> 2, rot = (rb + 2 * cb) & 7;
  const u32 vx = (u32)(((lane >> 3) * DM + ((lane & 7) ^ (lane >> 3)) * 8) * 2);
  const int l12 = (lane * 43) >> 9, c12 = (lane - l12 * 12) ^ ((w & 1) << 1), l14 = (lane * 37) >> 9;
  const u32 vq = (u32)((l12 * 1536 + (c12 < 8 ? cb * 64 + c12 * 8 : 1024 - 64 + cb * 32 + c12 * 8)) * 2);
  const u32 v1 = (u32)((l14 * FF + (lane - l14 * 14) * 8) * 2);
  const rsrc_t rx = __builtin_amdgcn_make_buffer_rsrc((void*)x, 0, 2048 * DM * 2, 0x00020000);
  const rsrc_t rq = __builtin_amdgcn_make_buffer_rsrc((void*)wqkv, 0, DM * 1536 * 2, 0x00020000);
  const rsrc_t r1 = __builtin_amdgcn_make_buffer_rsrc((void*)w1, 0, DM * FF * 2, 0x00020000);
  const u32 xs = (u32)((rb * 128 + 16 * w) * (DM * 2)), qs = (u32)((8 * w) * (1536 * 2)), fs = (u32)((((w >> 1) * 16 + (w & 1) * 4) * FF + cb * 112) * 2);
#define WPOS(s) ((s) < 12 ? L_W + ((s) & 3) * WSLOT : (s) < 14 ? L_R + ((s) - 12) * WSLOT : L_W + ((s) - 12) * WSLOT)
#define ISSUE_A(s) do { const int kc = ((s) + rot) & 7; \
    if ((s) < 8) { \
      dma16(rx, vx, xs + kc * 128, lds0 + L_X + ((s) & 3) * 16384 + w * 2048); \
      dma16(rx, vx, xs + kc * 128 + 8 * DM * 2, lds0 + L_X + ((s) & 3) * 16384 + w * 2048 + 1024); \
    } else if ((s) < 16) { \
      dma16m(r1, v1, fs + kc * (64 * FF * 2), lds0 + WPOS(s) + w * (8 * 224), 0x00FFFFFFFFFFFFFFull); \
    } } while (0)
#define ISSUE_B(s) do { const int kc = ((s) + rot) & 7; \
    if ((s) < 8) { \
      dma16m(rq, vq, qs + kc * (64 * 1536 * 2), lds0 + L_W + ((s) & 3) * WSLOT + w * (8 * 192), 0x0000FFFFFFFFFFFFull); \
      dma16m(rq, vq, qs + kc * (64 * 1536 * 2) + 4 * 1536 * 2, lds0 + L_W + ((s) & 3) * WSLOT + w * (8 * 192) + 4 * 192, 0x0000FFFFFFFFFFFFull); \
    } else if ((s) < 16) { \
      dma16m(r1, v1, fs + kc * (64 * FF * 2) + 8 * FF * 2, lds0 + WPOS(s) + w * (8 * 224) + 4 * 224, 0x00FFFFFFFFFFFFFFull); \
    } } while (0)
  ISSUE_A(0); ISSUE_B(0); STAMP(1); ISSUE_A(1); ISSUE_B(1); ISSUE_A(2); ISSUE_B(2);
  u32x2 epoch;
  asm volatile("global_load_dwordx2 %0, %1, %2 sc1" : "=v"(epoch) : "v"(WS_EPOCH), "s"(ws) : "memory");
#define NW0 9
  STAMP(13);
  u32x4 xf[2][16];
  f32x4 aq[3][2];
  bf16x8 wa[4], wb[4];
#pragma unroll
  for (int t = 0; t < 3; ++t) { aq[t][0] = f32x4{}; aq[t][1] = f32x4{}; }
  const lds_ptr xr0 = lds + L_X + (rg * 32 + r16) * 128 + ((g4 ^ (r16 & 7)) * 16), xr1 = lds + L_X + (rg * 32 + r16) * 128 + (((4 + g4) ^ (r16 & 7)) * 16);
  const lds_ptr wqb = lds + L_W + (8 * g4 + (r16 >> 2)) * 192 + (r16 & 3) * 8;
  const lds_ptr wq0 = wqb + ((2 * ch) ^ (g4 & 1)) * 32, wq1 = wqb + ((2 * ch + 1) ^ (g4 & 1)) * 32, wq2 = wqb + ((4 + ch) ^ (g4 & 1)) * 32;
#define QRD(F, s, ks) do { const int o = ((s) & 3) * WSLOT + (ks) * (32 * 192); \
    F[0] = trfrag(wq0 + o, wq0 + o + 4 * 192); F[1] = trfrag(wq1 + o, wq1 + o + 4 * 192); F[2] = trfrag(wq2 + o, wq2 + o + 4 * 192); } while (0)
#define QMM(F, kk, rt) do { const bf16x8 xv = asbf(xf[rt][kk]); \
    aq[0][rt] = MFMA16(F[0], xv, aq[0][rt]); aq[1][rt] = MFMA16(F[1], xv, aq[1][rt]); aq[2][rt] = MFMA16(xv, F[2], aq[2][rt]); \
    KEEP(aq[0][rt]); KEEP(aq[1][rt]); KEEP(aq[2][rt]); SBAR(); } while (0)
#define QSTAGE(s, n) do { WAITB(n); STAMP(16 + (s)); \
    xf[0][2 * (s)] = lds_ld16(xr0 + ((s) & 3) * 16384); xf[1][2 * (s)] = lds_ld16(xr0 + ((s) & 3) * 16384 + 2048); QRD(wa, s, 0); SBAR(); \
    if ((s) > 0) QMM(wb, 2 * (s) - 1, 0); \
    ISSUE_A((s) + 3); SBAR(); \
    if ((s) > 0) QMM(wb, 2 * (s) - 1, 1); \
    xf[0][2 * (s) + 1] = lds_ld16(xr1 + ((s) & 3) * 16384); xf[1][2 * (s) + 1] = lds_ld16(xr1 + ((s) & 3) * 16384 + 2048); QRD(wb, s, 1); SBAR(); \
    QMM(wa, 2 * (s), 0); \
    ISSUE_B((s) + 3); SBAR(); \
    QMM(wa, 2 * (s), 1); } while (0)
  QSTAGE(0, NW0); QSTAGE(1, NW0); QSTAGE(2, NW0); QSTAGE(3, 8);
  PIN(epoch);
  PIN(cpf);
  u64 tag = (((u64)__builtin_amdgcn_readfirstlane(epoch[1]) << 32) | (u64)__builtin_amdgcn_readfirstlane(epoch[0])) + 1;
  asm volatile("" : "+s"(tag));
  QSTAGE(4, 8); QSTAGE(5, 8); QSTAGE(6, 6); QSTAGE(7, 4);
  f32x4 ah[4][2];
#pragma unroll
  for (int c = 0; c < 4; ++c) { ah[c][0] = f32x4{}; ah[c][1] = f32x4{}; }
  const lds_ptr w1b = lds + (16 * (g4 >> 1) + 4 * (g4 & 1) + (r16 >> 2)) * 224 + (r16 & 3) * 8 + ch * 128;
#define FRD(F, s, ks) do { const lds_ptr p = w1b + WPOS(s) + (ks) * (32 * 224); \
    F[0] = trfrag(p, p + 8 * 224); F[1] = trfrag(p + 32, p + 32 + 8 * 224); F[2] = trfrag(p + 64, p + 64 + 8 * 224); \
    if (ch == 0) F[3] = trfrag(p + 96, p + 96 + 8 * 224); } while (0)
#define FMM(F, kk, rt) do { const bf16x8 xv = asbf(xf[rt][kk]); \
    ah[0][rt] = MFMA16(F[0], xv, ah[0][rt]); ah[1][rt] = MFMA16(F[1], xv, ah[1][rt]); ah[2][rt] = MFMA16(F[2], xv, ah[2][rt]); \
    KEEP(ah[0][rt]); KEEP(ah[1][rt]); KEEP(ah[2][rt]); \
    if (ch == 0) { ah[3][rt] = MFMA16(F[3], xv, ah[3][rt]); KEEP(ah[3][rt]); } SBAR(); } while (0)
#define KVDMA(j) do { const int sl_ = ((j) >> 1) * 4 + (w >> 1) + 32 * ((j) & 1); \
    dma16c(rs, lane * 16, WS_KV + head * 131072 + ((sl_ + qb * 8) & 63) * 2048 + (w & 1) * 1024, lds0 + sl_ * KVS + (w & 1) * 1024); } while (0)
#define FBODY(s, M0, M1, END) do { \
    FRD(wa, s, 0); SBAR(); \
    if ((s) > 8) FMM(wb, 2 * ((s) - 8) - 1, 0); \
    M0; SBAR(); \
    if ((s) > 8) FMM(wb, 2 * ((s) - 8) - 1, 1); \
    FRD(wb, s, 1); SBAR(); \
    FMM(wa, 2 * ((s) - 8), 0); \
    M1; SBAR(); \
    FMM(wa, 2 * ((s) - 8), 1); \
    END; } while (0)
#define FSTAGE(s, n, M0, M1, END) do { WAITB(n); STAMP(16 + (s)); FBODY(s, M0, M1, END); } while (0)
  WAITB(4); STAMP(24);
  ISSUE_A(11); ISSUE_B(11); ISSUE_A(12); ISSUE_B(12); ISSUE_A(13); ISSUE_B(13); SBAR();
  QMM(wb, 15, 0); QMM(wb, 15, 1);
  STAMP(2);
  {
    const int tile = rb * 4 + rg;
    const float qsc = cb < 8 ? QSCALE : 1.0f;
    const u32 qkb = (cb < 8 ? WS_Q + (u32)(((cb * 4 + 2 * ch) * 64 + tile) * 1024) : WS_KV + (u32)(((cb - 8) * 4 + 2 * ch) * 131072 + tile * 2048)) + lane * 16;
    const u32 hstride = cb < 8 ? 65536u : 131072u;
    ST16(pair16(aq[0][0] * qsc, aq[0][1] * qsc), qkb);
    ST16(pair16(aq[1][0] * qsc, aq[1][1] * qsc), qkb + hstride);
    ST16(pair32(aq[2][0], aq[2][1]), WS_KV + (cb * 2 + ch) * 131072 + tile * 2048 + 1024 + lane * 16);
    ST16((u32x4{~0u, ~0u, ~0u, ~0u}), WS_O + ((qb * 8 + w) * 32 + head) * 1024 + lane * 16);
  }
  STAMP(3);
  FBODY(8, , , );
  FSTAGE(9, 12, , , );
  FSTAGE(10, 0, , , );
  if (tid == 0) __hip_atomic_store((GAS u64*)(ws + WS_FLAG1 + b * 8), tag, RLX_AGENT);
  STAMP(4);
  FSTAGE(11, 63, ISSUE_A(14), ISSUE_B(14), );
  const int grow = rb * 128 + cb * 8 + w;
  u32x4 gx = *(const u32x4*)(x + grow * DM + lane * 8), gv = *(const u32x4*)(gw + lane * 8);
  u32x2 b1v[4];
#pragma unroll
  for (int c = 0; c < 4; ++c) b1v[c] = *(const u32x2*)(b1 + cb * 112 + (c == 3 ? 48 : ch * 64 + c * 16) + g4 * 4);
  const int pu = b + 256 * w;
  const bool pdo = pu < 1152, pw2 = pu < 896;
  const int prow = (pw2 ? pu : pu - 896) >> 3, pcg = pu & 7;
#define PTLOAD() do { if (pdo) { \
      const rsrc_t rp = __builtin_amdgcn_make_buffer_rsrc((void*)(pw2 ? w2 : wo), 0, (pw2 ? FF : DM) * DM * 2, 0x00020000); \
      const u32 vo = (u32)((lane >> 3) * (DM * 2) + (lane & 7) * 16), so = (u32)(prow * 16 * (DM * 2) + pcg * 128); \
      dma16(rp, vo, so, lds0 + L_PT + w * 2048); dma16(rp, vo, so + 8 * DM * 2, lds0 + L_PT + w * 2048 + 1024); } } while (0)
#define PTSTORE() do { if (pdo) { \
      const lds_ptr p = lds + L_PT + w * 2048 + ((pw2 ? 8 : 4) * hh + (r16 >> 2)) * 128 + (g4 & 1) * 32 + (r16 & 3) * 8; const int d = pw2 ? 512 : 1024; \
      const u32 o = (pw2 ? WS_W2F + (u32)((pcg * 2 * 112 + prow) * 1024) : WS_WOF + (u32)((pcg * 2 * 32 + prow) * 1024)) + lane * 16; \
      ST16(__builtin_bit_cast(u32x4, trfrag(p, p + d)), o); ST16(__builtin_bit_cast(u32x4, trfrag(p + 64, p + 64 + d)), o + (pw2 ? 112 : 32) * 1024); } } while (0)
  const int prb = lane < 32 ? r16 : 2 * qb + (lane & 1), pcb = lane < 16 ? 8 + (head >> 2) : lane < 32 ? (head >> 1) : (head >> 2);
  const u32 f1off = WS_FLAG1 + (((pcb >> 2) << 1 | (prb >> 3)) | (((pcb & 3) << 3 | (prb & 7)) << 3)) * 8;
  const int qg = w & 3, kh = w >> 2;
  u64 f1v;
  u32x4 qAu, qBu;
#define F1LOAD() asm volatile("global_load_dwordx2 %0, %1, %2 sc1" : "=v"(f1v) : "v"(f1off), "s"(ws) : "memory")
#define F1CHECK() do { \
    asm volatile("s_waitcnt vmcnt(0)" : "+v"(f1v) :: "memory"); \
    asm volatile("" : "+v"(gx), "+v"(gv), "+v"(b1v[0]), "+v"(b1v[1]), "+v"(b1v[2]), "+v"(b1v[3])); \
    for (u32 spins = 0; !__all(f1v == tag);) { \
      if (++spins > SPIN_CAP) { alive = false; break; } \
      __builtin_amdgcn_s_sleep(1); \
      f1v = __hip_atomic_load((GAS u64*)(ws + f1off), RLX_AGENT); } \
    asm volatile("" ::: "memory"); STAMP(6); \
    const u32 qo = WS_Q + (head * 64 + qb * 8 + qg * 2) * 1024 + lane * 16; \
    asm volatile("global_load_dwordx4 %0, %2, %3 sc1\n\tglobal_load_dwordx4 %1, %2, %3 offset:1024 sc1" : "=&v"(qAu), "=&v"(qBu) : "v"(qo), "s"(ws) : "memory"); \
    KVDMA(0); KVDMA(1); } while (0)
  PTLOAD();
  FSTAGE(12, 63, ISSUE_A(15), ISSUE_B(15), F1LOAD());
  FSTAGE(13, 63, , , F1CHECK());
  FSTAGE(14, 63, KVDMA(2); KVDMA(3), KVDMA(4); KVDMA(5), );
  FSTAGE(15, 63, KVDMA(6), KVDMA(7), PTSTORE());
  FMM(wb, 15, 0); FMM(wb, 15, 1);
  STAMP(5);
  BARRIER();
  if ((tid & 7) < 6) { const u32 one = ((tid & 3) == 0) ? 0x3F803F80u : 0u; lds_st16(lds + (tid >> 3) * KVS + 2048 + (tid & 7) * 16, u32x4{one, one, one, one}); }
  {
    const u32 hb = WS_H + (u32)(((rb * 4 + rg) * 112 + cb * 7 + ch * 4) * 1024) + lane * 16;
#pragma unroll
    for (int c = 0; c < 4; ++c) {
      if (c < 3 || ch == 0) {
        const f32x4 bb = unpk4(b1v[c]);
        ST16(pair16(gelu4(ah[c][0] + bb), gelu4(ah[c][1] + bb)), hb + c * 1024);
      }
    }
    float s = 0.f;
#pragma unroll
    for (int e = 0; e < 4; ++e) { const u32 ua = gx[e], ub = gv[e]; s = __builtin_amdgcn_fdot2_f32_bf16(__builtin_bit_cast(bf16x2, ua), __builtin_bit_cast(bf16x2, ub), s, false); }
    const float z = rbf(wave_sum(s)), g = rbf(__builtin_amdgcn_rcpf(1.0f + EX(z * -1.4426950408889634f)));
    if (lane == 0) __builtin_amdgcn_raw_buffer_store_b32(__float_as_uint(g), rs, WS_G + grow * 4, 0, AUX_SC1);
  }
  STAMP(7);
  asm volatile("s_waitcnt vmcnt(4) lgkmcnt(0)\n\ts_barrier" : "+v"(qAu), "+v"(qBu) :: "memory");
  const bf16x8 qA = asbf(qAu), qB = asbf(qBu);
  STAMP(8);

  lds_ptr kp = lds + kh * (32 * KVS) + lane * 16;
  const bool grp2 = (r32 >= 16 && r32 < 20) || r32 >= 28;
  const int coff = 2048 + (r32 == 16 ? 0 : r32 == 20 ? 64 : grp2 ? 16 : 80);
  lds_ptr vp0 = lds + kh * (32 * KVS) + (r32 < 16 ? 1024 + hh * 256 + r32 * 16 : coff), vp1 = lds + kh * (32 * KVS) + (r32 < 16 ? 1536 + hh * 256 + r32 * 16 : coff);
  f32x16 sA, sB, oA = splat16(0.f), oB = splat16(0.f), cn16;
  f32x16 fa[2][2];
  fa[0][0] = splat16(0.f); fa[0][1] = splat16(0.f); fa[1][0] = splat16(0.f); fa[1][1] = splat16(0.f);
  u32x4 kf, v0, v1f, p1 = u32x4{0, 0, 0, 0};
  u32x4 sh[2][2], sw[2][2];
  {
    kf = lds_ld16(kp); v0 = lds_ld16(vp0); v1f = lds_ld16(vp1);
    sA = MFMA32(asbf(kf), qA, splat16(0.f));
    sB = MFMA32(asbf(kf), qB, splat16(0.f));
    cn16 = splat16(-xhalf_max(__builtin_fmaxf(max16(sA), max16(sB))));
    PIN(cn16);
    sA = MFMA32(asbf(kf), qA, cn16);
  }
  const u32 hoff = WS_H + (u32)((tb * 2 * 112 + w * 14) * 1024) + lane * 16;
  const u32 woff = WS_W2F + (u32)((nb * 2 * 112 + w * 14) * 1024) + lane * 16;
  int fk = (tb & 7) + 4 * (nb & 3); fk = fk >= 14 ? fk - 14 : fk;
#define UNIT(SC, SN, QN, OC, OP, RD0, RD1, RD2, FMX, LD) do { \
    SN = MFMA32(asbf(kf), QN, cn16); KEEP(SN); RD0; \
    SBAR(); \
    SC[0] = EX(SC[0]); SC[1] = EX(SC[1]); SC[2] = EX(SC[2]); SC[3] = EX(SC[3]); PIN(SC); \
    SBAR(); \
    OP = MFMA32(asbf(v1f), asbf(p1), OP); KEEP(OP); RD1; \
    SBAR(); \
    u32x4 p0; \
    SC[4] = EX(SC[4]); SC[5] = EX(SC[5]); SC[6] = EX(SC[6]); SC[7] = EX(SC[7]); p0[0] = cvtpk(SC[0], SC[1]); p0[1] = cvtpk(SC[2], SC[3]); PIN(SC); PIN(p0); \
    SBAR(); \
    FMX; LD; \
    SBAR(); \
    p0[2] = cvtpk(SC[4], SC[5]); p0[3] = cvtpk(SC[6], SC[7]); PIN(p0); SBAR(); \
    SC[8] = EX(SC[8]); SC[9] = EX(SC[9]); SC[10] = EX(SC[10]); PIN(SC); \
    SBAR(); \
    OC = MFMA32(asbf(v0), asbf(p0), OC); KEEP(OC); RD2; \
    SBAR(); \
    SC[11] = EX(SC[11]); SC[12] = EX(SC[12]); SC[13] = EX(SC[13]); SC[14] = EX(SC[14]); SC[15] = EX(SC[15]); \
    p1 = pack8(SC, 8); PIN(p1); \
    SBAR(); } while (0)
#define UA(t, FMX, LD) UNIT(sA, sB, qB, oA, oB, kf = lds_ld16(kp + ((t) + 1) * KVS), v1f = lds_ld16(vp1 + (t) * KVS), , FMX, LD)
#define UB(t, FMX, LD) UNIT(sB, sA, qA, oB, oA, , , v0 = lds_ld16(vp0 + ((t) + 1) * KVS), FMX, LD)
#define ADV() do { kp += 4 * KVS; vp0 += 4 * KVS; vp1 += 4 * KVS; } while (0)
#define LDH(s, i, so) sh[s][i] = LD16(hoff, (so) + (i) * (112 * 1024))
#define LDW(s, i, so) sw[s][i] = LD16(woff, (so) + (i) * (112 * 1024))
#define FM(s, n, t) fa[n][t] = MFMA32(asbf(sw[s][n]), asbf(sh[s][t]), fa[n][t]); KEEP(fa[n][t])
#define NEXTK() __extension__({ const int o_ = fk * 1024; fk = fk == 13 ? 0 : fk + 1; o_; })
  UA(0, , KVDMA(8); KVDMA(9); KVDMA(10)); UB(0, , KVDMA(11); KVDMA(12); KVDMA(13)); UA(1, , KVDMA(14); KVDMA(15)); UB(1, , );
  WAITB(0);
  if (tid == 0) __hip_atomic_store((GAS u64*)(ws + WS_FLAG2 + b * 8), tag, RLX_AGENT);
  UA(2, , ); UB(2, , ); UA(3, , ); UB(3, , ); ADV();
  u32x4 f0, f1;
  UA(0, , ); UB(0, , f0 = LD16(WS_FLAG2 + lane * 32, 0); f1 = LD16(WS_FLAG2 + lane * 32 + 16, 0)); UA(1, , ); UB(1, , );
  for (u32 spins = 0; alive;) {
    const u32 lo = (u32)tag, hi = (u32)(tag >> 32);
    const bool ok = f0[0] == lo && f0[1] == hi && f0[2] == lo && f0[3] == hi && f1[0] == lo && f1[1] == hi && f1[2] == lo && f1[3] == hi;
    if (__all(ok)) break;
    if (++spins > SPIN_CAP) { alive = false; break; }
    __builtin_amdgcn_s_sleep(1);
    f0 = LD16(WS_FLAG2 + lane * 32, 0); f1 = LD16(WS_FLAG2 + lane * 32 + 16, 0);
  }
  asm volatile("" ::: "memory");
  if (b == 0 && tid == 0 && alive) __hip_atomic_store((GAS u64*)(ws + WS_EPOCH), tag, RLX_AGENT);
  STAMP(9);
  { const int s0 = NEXTK(), s1 = NEXTK();
    LDH(0, 0, s0); LDW(0, 0, s0); LDW(0, 1, s0); LDH(0, 1, s0); LDH(1, 0, s1); LDW(1, 0, s1); LDW(1, 1, s1); LDH(1, 1, s1); }
  const int tb2 = w & 1, kq = w >> 1;
  const float gq = __uint_as_float(__builtin_amdgcn_raw_buffer_load_b32(rs, WS_G + (qb * 256 + qg * 64 + kh * 32 + r32) * 4, 0, AUX_SC1));
  const float gt = __uint_as_float(__builtin_amdgcn_raw_buffer_load_b32(rs, WS_G + (tb * 64 + tb2 * 32 + r32) * 4, 0, AUX_SC1));
  UA(2, , ); UB(2, , ); UA(3, , ); UB(3, , ); ADV();
  for (int it = 2; it < 8; ++it) {
    const int s0 = NEXTK(), s1 = NEXTK();
    UA(0, FM(0, 0, 0), ); UB(0, FM(0, 1, 0), LDH(0, 0, s0)); UA(1, FM(0, 0, 1), LDW(0, 0, s0)); UB(1, FM(0, 1, 1), LDW(0, 1, s0); LDH(0, 1, s0));
    UA(2, FM(1, 0, 0), ); UB(2, FM(1, 1, 0), LDH(1, 0, s1)); UA(3, FM(1, 0, 1), LDW(1, 0, s1)); UB(3, FM(1, 1, 1), LDW(1, 1, s1); LDH(1, 1, s1));
    ADV();
  }
  oB = MFMA32(asbf(v1f), asbf(p1), oB);
  STAMP(10);
  float cA = cn16[0], cB = cA;
  if (__builtin_expect(!__all(oA[8] <= 0x1p30f && oB[8] <= 0x1p30f && oA[8] >= 0x1p-60f && oB[8] >= 0x1p-60f), 0)) {
    const lds_ptr k2 = kp - 32 * KVS, v2 = vp0 - 32 * KVS, v3 = vp1 - 32 * KVS;
#define REDO(Q, O, C) do { float m = -INFINITY; \
      for (int t = 0; t < 32; ++t) m = __builtin_fmaxf(m, max16(MFMA32(asbf(lds_ld16(k2 + t * KVS)), Q, splat16(0.f)))); \
      C = -xhalf_max(m); cn16 = splat16(C); O = splat16(0.f); \
      for (int t = 0; t < 32; ++t) { \
        f32x16 s = MFMA32(asbf(lds_ld16(k2 + t * KVS)), Q, cn16); \
        _Pragma("unroll") for (int r = 0; r < 16; ++r) s[r] = EX(s[r]); \
        O = MFMA32(asbf(lds_ld16(v2 + t * KVS)), asbf(pack8(s, 0)), O); O = MFMA32(asbf(lds_ld16(v3 + t * KVS)), asbf(pack8(s, 8)), O); } } while (0)
    REDO(qA, oA, cA); REDO(qB, oB, cB);
  }
  int lz;
  asm volatile("v_mbcnt_lo_u32_b32 %0, -1, 0\n\tv_mbcnt_hi_u32_b32 %0, -1, %0" : "=v"(lz));
  const int ncol = nb * 64 + kq * 16;
  const u32x2 bia = *(const u32x2*)(b2 + ncol + 4 * (lz >> 5)), bib = *(const u32x2*)(b2 + ncol + 8 + 4 * (lz >> 5));
  {
    const f32x16 os = kh ? oA : oB;
    const float cs = kh ? cA : cB;
    const lds_ptr p = lds + L_CMB + w * 2560 + lz * 16;
    lds_stf(p, f32x4{os[0], os[1], os[2], os[3]}); lds_stf(p + 1024, f32x4{os[4], os[5], os[6], os[7]});
    *(LAS f32x2*)(lds + L_CMB + w * 2560 + 2048 + lz * 8) = f32x2{os[8], cs};
  }
  BARRIER();
  {
    const f32x16 om = kh ? oB : oA;
    const float cm = kh ? cB : cA;
    const lds_ptr p = lds + L_CMB + (w ^ 4) * 2560 + lz * 16;
    const f32x4 a = lds_ldf(p), c = lds_ldf(p + 1024);
    const f32x2 lc = *(LAS f32x2*)(lds + L_CMB + (w ^ 4) * 2560 + 2048 + lz * 8);
    const float cn = __builtin_fminf(cm, lc[1]);
    float e0 = EX(cn - cm), e1 = EX(cn - lc[1]);
    const float sc = gq * __builtin_amdgcn_rcpf(om[8] * e0 + lc[0] * e1);
    e0 *= sc; e1 *= sc;
    u32x4 o = u32x4{cvtpk(om[0] * e0 + a[0] * e1, om[1] * e0 + a[1] * e1), cvtpk(om[2] * e0 + a[2] * e1, om[3] * e0 + a[3] * e1),
                    cvtpk(om[4] * e0 + c[0] * e1, om[5] * e0 + c[1] * e1), cvtpk(om[6] * e0 + c[2] * e1, om[7] * e0 + c[3] * e1)};
#pragma unroll
    for (int e = 0; e < 4; ++e) o[e] = (o[e] == ~0u) ? 0x7FC07FC0u : o[e];
    ST16(o, WS_O + ((qb * 8 + qg * 2 + kh) * 32 + head) * 1024 + lz * 16);
  }
  STAMP(14);
  SBAR();
  u32x4 wf[8][2], of[8];
#define OLOAD() _Pragma("unroll") for (int j = 0; j < 8; ++j) of[j] = LD16(WS_O + ((tb * 2 + tb2) * 32 + kq * 8 + j) * 1024 + lz * 16, 0)
#pragma unroll
  for (int j = 0; j < 8; ++j) {
    wf[j][0] = LD16(WS_WOF + ((nb * 2) * 32 + kq * 8 + j) * 1024 + lz * 16, 0);
    wf[j][1] = LD16(WS_WOF + ((nb * 2 + 1) * 32 + kq * 8 + j) * 1024 + lz * 16, 0);
  }
  FM(0, 0, 0); FM(0, 1, 0); FM(0, 0, 1); FM(0, 1, 1); FM(1, 0, 0); FM(1, 1, 0); FM(1, 0, 1); FM(1, 1, 1);
#define Q4(a, r) f32x4{(a)[r], (a)[(r) + 1], (a)[(r) + 2], (a)[(r) + 3]}
#pragma unroll
  for (int d = 0; d < 8; ++d) {
    const lds_ptr p = lds + (d * 8 + w) * 2048 + lz * 16;
    lds_stf(p, Q4(fa[d >> 2][d & 1], ((d >> 1) & 1) * 8));
    lds_stf(p + 1024, Q4(fa[d >> 2][d & 1], ((d >> 1) & 1) * 8 + 4));
  }
  OLOAD();
  BARRIER();
  f32x4 lo = unpk4(bia), hi = unpk4(bib);
#pragma unroll
  for (int s = 0; s < 8; ++s) {
    const lds_ptr p = lds + (w * 8 + s) * 2048 + lz * 16;
    lo += lds_ldf(p); hi += lds_ldf(p + 1024);
  }
  { const float og = rbf(1.0f - gt); lo *= og; hi *= og; }
  PIN(lo); PIN(hi);
  BARRIER();
  for (u32 spins = 0;;) {
    bool ok = true;
#pragma unroll
    for (int j = 0; j < 8; ++j) ok = ok && of[j][0] != ~0u && of[j][1] != ~0u && of[j][2] != ~0u && of[j][3] != ~0u;
    if (__all(ok) || !alive) break;
    if (++spins > SPIN_CAP) { alive = false; break; }
    asm volatile("" ::: "memory");
    OLOAD();
  }
  STAMP(11);
  f32x16 pa[2] = {splat16(0.f), splat16(0.f)};
#pragma unroll
  for (int j = 0; j < 8; ++j) { pa[0] = MFMA32(asbf(wf[j][0]), asbf(of[j]), pa[0]); pa[1] = MFMA32(asbf(wf[j][1]), asbf(of[j]), pa[1]); }
#pragma unroll
  for (int q = 0; q < 4; ++q) {
    const lds_ptr p = lds + ((q * 2 + tb2) * 4 + kq) * 2048 + lz * 16;
    lds_stf(p, Q4(pa[q >> 1], (q & 1) * 8));
    lds_stf(p + 1024, Q4(pa[q >> 1], (q & 1) * 8 + 4));
  }
  BARRIER();
#pragma unroll
  for (int s = 0; s < 4; ++s) {
    const lds_ptr p = lds + (w * 4 + s) * 2048 + lz * 16;
    lo += lds_ldf(p); hi += lds_ldf(p + 1024);
  }
  *(u32x4*)(out + (tb * 64 + tb2 * 32 + (lz & 31)) * DM + ncol + 8 * (lz >> 5)) = pair32(lo, hi);
  STAMP(12);
}

extern "C" void kernel_launch(void* const* d_in, const int* in_sizes, int n_in, void* d_out, int out_size, void* d_ws, size_t ws_size,
                              hipStream_t stream) {
  const u16 *x = (const u16*)d_in[0], *wqkv = (const u16*)d_in[1], *wo = (const u16*)d_in[2], *w1 = (const u16*)d_in[4],
            *b1 = (const u16*)d_in[5], *w2 = (const u16*)d_in[6], *b2 = (const u16*)d_in[7], *gw = (const u16*)d_in[8];
  fused_kernel<<<256, 512, 0, stream>>>(x, wqkv, wo, w1, b1, w2, b2, gw, (u16*)d_out, (char*)d_ws);
}
```

```cpp
#define NO_CODEPF
#include <hip/hip_runtime.h>
#include <hip/hip_bf16.h>
#include <stdint.h>

typedef unsigned short u16;
typedef unsigned int u32;
typedef unsigned long long u64;
typedef __attribute__((ext_vector_type(8))) __bf16 bf16x8;
typedef __attribute__((ext_vector_type(2))) __bf16 bf16x2;
typedef __attribute__((ext_vector_type(4))) float f32x4;
typedef __attribute__((ext_vector_type(2))) float f32x2;
typedef __attribute__((ext_vector_type(16))) float f32x16;
typedef __attribute__((ext_vector_type(4))) u32 u32x4;
typedef __attribute__((ext_vector_type(2))) u32 u32x2;
#define LAS __attribute__((address_space(3)))
#define GAS __attribute__((address_space(1)))
typedef LAS char* lds_ptr;

constexpr int DM = 512, FF = 1792;
constexpr u32 WS_EPOCH = 0, WS_FLAG1 = 4096, WS_FLAG2 = 8192, WS_G = 16384, WS_STAMP = 20u << 20;
constexpr u32 WS_Q = 1u << 20, WS_KV = 3u << 20, WS_H = 7u << 20, WS_W2F = 14u << 20, WS_WOF = 16u << 20, WS_O = 17u << 20, WS_END = 19u << 20;
constexpr int L_X = 0, L_W = 104 * 1024, WSLOT = 14336, KVS = 2176, L_CMB = 64 * KVS, L_PT = L_W, L_R = 16 * KVS, LDS_BYTES = 160 * 1024;
constexpr int AUX_SC1 = 16;
constexpr int CODE_LINES = 160;
constexpr u32 SPIN_CAP = 1u << 15;
constexpr float QSCALE = 0.25f * 1.4426950408889634f;

#define SBAR() __builtin_amdgcn_sched_barrier(0)
#define PIN(x) asm volatile("" : "+v"(x))
#define KEEP(x) asm volatile("" :: "v"(x))
#define MFMA32(a, b, c) __builtin_amdgcn_mfma_f32_32x32x16_bf16(a, b, c, 0, 0, 0)
#define MFMA16(a, b, c) __builtin_amdgcn_mfma_f32_16x16x32_bf16(a, b, c, 0, 0, 0)
#define BARRIER() asm volatile("s_waitcnt lgkmcnt(0)\n\ts_barrier" ::: "memory")
#define WAITB_(n) asm volatile("s_waitcnt vmcnt(" #n ") lgkmcnt(0)\n\ts_barrier" ::: "memory")
#define WAITB(n) WAITB_(n)
#define RLX_AGENT __ATOMIC_RELAXED, __HIP_MEMORY_SCOPE_AGENT
#define EX(v) __builtin_amdgcn_exp2f(v)
#define LD16(off, soff) __builtin_amdgcn_raw_buffer_load_b128(rs, off, soff, AUX_SC1)
#define ST16(v, off) __builtin_amdgcn_raw_buffer_store_b128(v, rs, off, 0, AUX_SC1)

#define STAMP(i) do { } while (0)

__device__ __forceinline__ float bf2f(u32 v) { return __uint_as_float(v << 16); }
__device__ __forceinline__ float bfhi(u32 v) { return __uint_as_float(v & 0xffff0000u); }
__device__ __forceinline__ u32 cvtpk(float lo, float hi) { bf16x2 r = {(__bf16)lo, (__bf16)hi}; return __builtin_bit_cast(u32, r); }
__device__ __forceinline__ float rbf(float f) { return bf2f(cvtpk(f, f)); }
__device__ __forceinline__ bf16x8 asbf(u32x4 v) { return __builtin_bit_cast(bf16x8, v); }
typedef __amdgpu_buffer_rsrc_t rsrc_t;
__device__ __forceinline__ void dma16(rsrc_t r, u32 voff, u32 soff, u32 lds_base) {
  asm volatile("s_mov_b32 m0, %3\n\ts_nop 0\n\tbuffer_load_dwordx4 %0, %1, %2 offen lds" :: "v"(voff), "s"(r), "s"(soff), "s"(lds_base) : "memory");
}
__device__ __forceinline__ void dma16c(rsrc_t r, u32 voff, u32 soff, u32 lds_base) {
  asm volatile("s_mov_b32 m0, %3\n\ts_nop 0\n\tbuffer_load_dwordx4 %0, %1, %2 offen sc1 lds" :: "v"(voff), "s"(r), "s"(soff), "s"(lds_base) : "memory");
}
__device__ __forceinline__ void dma16m(rsrc_t r, u32 voff, u32 soff, u32 lds_base, u64 mask) {
  asm volatile("s_mov_b32 m0, %3\n\ts_mov_b64 exec, %4\n\tbuffer_load_dwordx4 %0, %1, %2 offen lds\n\ts_mov_b64 exec, -1"
               :: "v"(voff), "s"(r), "s"(soff), "s"(lds_base), "s"(mask) : "memory");
}
__device__ __forceinline__ bf16x8 trfrag(lds_ptr p0, lds_ptr p1) {
  typedef short v4i16_t __attribute__((ext_vector_type(4)));
  typedef short v8i16_t __attribute__((ext_vector_type(8)));
  v4i16_t a = __builtin_amdgcn_ds_read_tr16_b64_v4i16((LAS v4i16_t*)p0);
  v4i16_t b = __builtin_amdgcn_ds_read_tr16_b64_v4i16((LAS v4i16_t*)p1);
  v8i16_t r = __builtin_shufflevector(a, b, 0, 1, 2, 3, 4, 5, 6, 7);
  return __builtin_bit_cast(bf16x8, r);
}
__device__ __forceinline__ u32x4 lds_ld16(lds_ptr p) { return *(LAS u32x4*)p; }
__device__ __forceinline__ f32x4 lds_ldf(lds_ptr p) { return *(LAS f32x4*)p; }
__device__ __forceinline__ void lds_st16(lds_ptr p, u32x4 v) { *(LAS u32x4*)p = v; }
__device__ __forceinline__ void lds_stf(lds_ptr p, f32x4 v) { *(LAS f32x4*)p = v; }
__device__ __forceinline__ u32x4 pair16(f32x4 a, f32x4 b) {
  u32 a0 = cvtpk(a[0], a[1]), a1 = cvtpk(a[2], a[3]), b0 = cvtpk(b[0], b[1]), b1 = cvtpk(b[2], b[3]);
  auto s0 = __builtin_amdgcn_permlane16_swap(a0, b0, false, false);
  auto s1 = __builtin_amdgcn_permlane16_swap(a1, b1, false, false);
  return u32x4{s0[0], s1[0], s0[1], s1[1]};
}
__device__ __forceinline__ u32x4 pair32(f32x4 a, f32x4 b) {
  u32 a0 = cvtpk(a[0], a[1]), a1 = cvtpk(a[2], a[3]), b0 = cvtpk(b[0], b[1]), b1 = cvtpk(b[2], b[3]);
  auto s0 = __builtin_amdgcn_permlane32_swap(a0, b0, false, false);
  auto s1 = __builtin_amdgcn_permlane32_swap(a1, b1, false, false);
  return u32x4{s0[0], s1[0], s0[1], s1[1]};
}
__device__ __forceinline__ f32x2 gelu2(f32x2 x) {
  constexpr float K0 = -2.0f * 0.7978845608028654f * 1.4426950408889634f, K1 = K0 * 0.044715f;
  const f32x2 z = x * __builtin_elementwise_fma(x * x, f32x2{K1, K1}, f32x2{K0, K0});
  const f32x2 d = f32x2{EX(z[0]), EX(z[1])} + 1.0f;
  return x * f32x2{__builtin_amdgcn_rcpf(d[0]), __builtin_amdgcn_rcpf(d[1])};
}
__device__ __forceinline__ f32x4 gelu4(f32x4 x) {
  const f32x2 a = gelu2(f32x2{x[0], x[1]}), c = gelu2(f32x2{x[2], x[3]});
  return f32x4{a[0], a[1], c[0], c[1]};
}
__device__ __forceinline__ f32x4 unpk4(u32x2 v) { return f32x4{bf2f(v[0]), bfhi(v[0]), bf2f(v[1]), bfhi(v[1])}; }
__device__ __forceinline__ float xhalf_max(float m) {
  auto r = __builtin_amdgcn_permlane32_swap(__float_as_uint(m), __float_as_uint(m), false, false);
  return __builtin_fmaxf(__uint_as_float(r[0]), __uint_as_float(r[1]));
}
__device__ __forceinline__ float max16(const f32x16& s) {
  float a = __builtin_fmaxf(s[0], s[1]), b = __builtin_fmaxf(s[2], s[3]);
#pragma unroll
  for (int r = 4; r < 16; r += 4) { a = __builtin_fmaxf(__builtin_fmaxf(a, s[r]), s[r + 1]); b = __builtin_fmaxf(__builtin_fmaxf(b, s[r + 2]), s[r + 3]); }
  return __builtin_fmaxf(a, b);
}
__device__ __forceinline__ f32x16 splat16(float v) { return f32x16{v, v, v, v, v, v, v, v, v, v, v, v, v, v, v, v}; }
template <int CTRL> __device__ __forceinline__ float dppadd(float s) {
  return s + __uint_as_float(__builtin_amdgcn_update_dpp(0u, __float_as_uint(s), CTRL, 0xf, 0xf, true));
}
__device__ __forceinline__ float wave_sum(float s) {
  s = dppadd<0x128>(s); s = dppadd<0x124>(s); s = dppadd<0x122>(s); s = dppadd<0x121>(s);
  auto a = __builtin_amdgcn_permlane16_swap(__float_as_uint(s), __float_as_uint(s), false, false);
  s = __uint_as_float(a[0]) + __uint_as_float(a[1]);
  auto c = __builtin_amdgcn_permlane32_swap(__float_as_uint(s), __float_as_uint(s), false, false);
  return __uint_as_float(c[0]) + __uint_as_float(c[1]);
}
__device__ __forceinline__ u32x4 pack8(const f32x16& s, int r) {
  return u32x4{cvtpk(s[r], s[r + 1]), cvtpk(s[r + 2], s[r + 3]), cvtpk(s[r + 4], s[r + 5]), cvtpk(s[r + 6], s[r + 7])};
}

__global__ void __launch_bounds__(512, 2)
fused_kernel(const u16* __restrict__ x, const u16* __restrict__ wqkv, const u16* __restrict__ wo, const u16* __restrict__ w1,
             const u16* __restrict__ b1, const u16* __restrict__ w2, const u16* __restrict__ b2, const u16* __restrict__ gw,
             u16* __restrict__ out, char* ws) {
  __shared__ __attribute__((aligned(16))) char smem[LDS_BYTES];
  const lds_ptr lds = (lds_ptr)smem;
  const u32 lds0 = (u32)(uintptr_t)lds;
  const int tid = threadIdx.x, lane = tid & 63, w = __builtin_amdgcn_readfirstlane(tid >> 6);
  const int b = blockIdx.x, xc = b & 7, bi = b >> 3;
  const int r16 = lane & 15, g4 = lane >> 4, r32 = lane & 31, hh = lane >> 5;
  const rsrc_t rs = __builtin_amdgcn_make_buffer_rsrc(ws, 0, WS_END, 0x00020000);
  const int rb = (xc & 1) * 8 + (bi & 7), cb = (xc >> 1) * 4 + (bi >> 3);
  const int head = xc * 4 + (bi >> 3), qb = bi & 7;
  const int tb = (xc & 3) * 8 + (bi & 7), nb = (xc >> 2) * 4 + (bi >> 3);
  bool alive = true;
  STAMP(0);

  const int rg = w & 3, ch = w >> 2, rot = (rb + 2 * cb) & 7;
  const u32 vx = (u32)(((lane >> 3) * DM + ((lane & 7) ^ (lane >> 3)) * 8) * 2);
  const int l12 = (lane * 43) >> 9, c12 = (lane - l12 * 12) ^ ((w & 1) << 1), l14 = (lane * 37) >> 9;
  const u32 vq = (u32)((l12 * 1536 + (c12 < 8 ? cb * 64 + c12 * 8 : 1024 - 64 + cb * 32 + c12 * 8)) * 2);
  const u32 v1 = (u32)((l14 * FF + (lane - l14 * 14) * 8) * 2);
  const rsrc_t rx = __builtin_amdgcn_make_buffer_rsrc((void*)x, 0, 2048 * DM * 2, 0x00020000);
  const rsrc_t rq = __builtin_amdgcn_make_buffer_rsrc((void*)wqkv, 0, DM * 1536 * 2, 0x00020000);
  const rsrc_t r1 = __builtin_amdgcn_make_buffer_rsrc((void*)w1, 0, DM * FF * 2, 0x00020000);
  const u32 xs = (u32)((rb * 128 + 16 * w) * (DM * 2)), qs = (u32)((8 * w) * (1536 * 2)), fs = (u32)((((w >> 1) * 16 + (w & 1) * 4) * FF + cb * 112) * 2);
#define WPOS(s) ((s) < 12 ? L_W + ((s) & 3) * WSLOT : (s) < 14 ? L_R + ((s) - 12) * WSLOT : L_W + ((s) - 12) * WSLOT)
#define ISSUE_A(s) do { const int kc = ((s) + rot) & 7; \
    if ((s) < 8) { \
      dma16(rx, vx, xs + kc * 128, lds0 + L_X + ((s) & 3) * 16384 + w * 2048); \
      dma16(rx, vx, xs + kc * 128 + 8 * DM * 2, lds0 + L_X + ((s) & 3) * 16384 + w * 2048 + 1024); \
    } else if ((s) < 16) { \
      dma16m(r1, v1, fs + kc * (64 * FF * 2), lds0 + WPOS(s) + w * (8 * 224), 0x00FFFFFFFFFFFFFFull); \
    } } while (0)
#define ISSUE_B(s) do { const int kc = ((s) + rot) & 7; \
    if ((s) < 8) { \
      dma16m(rq, vq, qs + kc * (64 * 1536 * 2), lds0 + L_W + ((s) & 3) * WSLOT + w * (8 * 192), 0x0000FFFFFFFFFFFFull); \
      dma16m(rq, vq, qs + kc * (64 * 1536 * 2) + 4 * 1536 * 2, lds0 + L_W + ((s) & 3) * WSLOT + w * (8 * 192) + 4 * 192, 0x0000FFFFFFFFFFFFull); \
    } else if ((s) < 16) { \
      dma16m(r1, v1, fs + kc * (64 * FF * 2) + 8 * FF * 2, lds0 + WPOS(s) + w * (8 * 224) + 4 * 224, 0x00FFFFFFFFFFFFFFull); \
    } } while (0)
  ISSUE_A(0); ISSUE_B(0); STAMP(1); ISSUE_A(1); ISSUE_B(1); ISSUE_A(2); ISSUE_B(2);
  u32x2 epoch;
  asm volatile("global_load_dwordx2 %0, %1, %2 sc1" : "=v"(epoch) : "v"(WS_EPOCH), "s"(ws) : "memory");
#define NW0 9
  STAMP(13);
  u32x4 xf[2][16];
  f32x4 aq[3][2];
  bf16x8 wa[4], wb[4];
#pragma unroll
  for (int t = 0; t < 3; ++t) { aq[t][0] = f32x4{}; aq[t][1] = f32x4{}; }
  const lds_ptr xr0 = lds + L_X + (rg * 32 + r16) * 128 + ((g4 ^ (r16 & 7)) * 16), xr1 = lds + L_X + (rg * 32 + r16) * 128 + (((4 + g4) ^ (r16 & 7)) * 16);
  const lds_ptr wqb = lds + L_W + (8 * g4 + (r16 >> 2)) * 192 + (r16 & 3) * 8;
  const lds_ptr wq0 = wqb + ((2 * ch) ^ (g4 & 1)) * 32, wq1 = wqb + ((2 * ch + 1) ^ (g4 & 1)) * 32, wq2 = wqb + ((4 + ch) ^ (g4 & 1)) * 32;
#define QRD(F, s, ks) do { const int o = ((s) & 3) * WSLOT + (ks) * (32 * 192); \
    F[0] = trfrag(wq0 + o, wq0 + o + 4 * 192); F[1] = trfrag(wq1 + o, wq1 + o + 4 * 192); F[2] = trfrag(wq2 + o, wq2 + o + 4 * 192); } while (0)
#define QMM(F, kk, rt) do { const bf16x8 xv = asbf(xf[rt][kk]); \
    aq[0][rt] = MFMA16(F[0], xv, aq[0][rt]); aq[1][rt] = MFMA16(F[1], xv, aq[1][rt]); aq[2][rt] = MFMA16(xv, F[2], aq[2][rt]); \
    KEEP(aq[0][rt]); KEEP(aq[1][rt]); KEEP(aq[2][rt]); SBAR(); } while (0)
#define QSTAGE(s, n) do { WAITB(n); STAMP(16 + (s)); \
    xf[0][2 * (s)] = lds_ld16(xr0 + ((s) & 3) * 16384); xf[1][2 * (s)] = lds_ld16(xr0 + ((s) & 3) * 16384 + 2048); QRD(wa, s, 0); SBAR(); \
    if ((s) > 0) QMM(wb, 2 * (s) - 1, 0); \
    ISSUE_A((s) + 3); SBAR(); \
    if ((s) > 0) QMM(wb, 2 * (s) - 1, 1); \
    xf[0][2 * (s) + 1] = lds_ld16(xr1 + ((s) & 3) * 16384); xf[1][2 * (s) + 1] = lds_ld16(xr1 + ((s) & 3) * 16384 + 2048); QRD(wb, s, 1); SBAR(); \
    QMM(wa, 2 * (s), 0); \
    ISSUE_B((s) + 3); SBAR(); \
    QMM(wa, 2 * (s), 1); } while (0)
  QSTAGE(0, NW0); QSTAGE(1, NW0); QSTAGE(2, NW0); QSTAGE(3, 8);
  PIN(epoch);
  u64 tag = (((u64)__builtin_amdgcn_readfirstlane(epoch[1]) << 32) | (u64)__builtin_amdgcn_readfirstlane(epoch[0])) + 1;
  asm volatile("" : "+s"(tag));
  QSTAGE(4, 8); QSTAGE(5, 8); QSTAGE(6, 6); QSTAGE(7, 4);
  f32x4 ah[4][2];
#pragma unroll
  for (int c = 0; c < 4; ++c) { ah[c][0] = f32x4{}; ah[c][1] = f32x4{}; }
  const lds_ptr w1b = lds + (16 * (g4 >> 1) + 4 * (g4 & 1) + (r16 >> 2)) * 224 + (r16 & 3) * 8 + ch * 128;
#define FRD(F, s, ks) do { const lds_ptr p = w1b + WPOS(s) + (ks) * (32 * 224); \
    F[0] = trfrag(p, p + 8 * 224); F[1] = trfrag(p + 32, p + 32 + 8 * 224); F[2] = trfrag(p + 64, p + 64 + 8 * 224); \
    if (ch == 0) F[3] = trfrag(p + 96, p + 96 + 8 * 224); } while (0)
#define FMM(F, kk, rt) do { const bf16x8 xv = asbf(xf[rt][kk]); \
    ah[0][rt] = MFMA16(F[0], xv, ah[0][rt]); ah[1][rt] = MFMA16(F[1], xv, ah[1][rt]); ah[2][rt] = MFMA16(F[2], xv, ah[2][rt]); \
    KEEP(ah[0][rt]); KEEP(ah[1][rt]); KEEP(ah[2][rt]); \
    if (ch == 0) { ah[3][rt] = MFMA16(F[3], xv, ah[3][rt]); KEEP(ah[3][rt]); } SBAR(); } while (0)
#define KVDMA(j) do { const int sl_ = ((j) >> 1) * 4 + (w >> 1) + 32 * ((j) & 1); \
    dma16c(rs, lane * 16, WS_KV + head * 131072 + ((sl_ + qb * 8) & 63) * 2048 + (w & 1) * 1024, lds0 + sl_ * KVS + (w & 1) * 1024); } while (0)
#define FBODY(s, M0, M1, END) do { \
    FRD(wa, s, 0); SBAR(); \
    if ((s) > 8) FMM(wb, 2 * ((s) - 8) - 1, 0); \
    M0; SBAR(); \
    if ((s) > 8) FMM(wb, 2 * ((s) - 8) - 1, 1); \
    FRD(wb, s, 1); SBAR(); \
    FMM(wa, 2 * ((s) - 8), 0); \
    M1; SBAR(); \
    FMM(wa, 2 * ((s) - 8), 1); \
    END; } while (0)
#define FSTAGE(s, n, M0, M1, END) do { WAITB(n); STAMP(16 + (s)); FBODY(s, M0, M1, END); } while (0)
  WAITB(4); STAMP(24);
  ISSUE_A(11); ISSUE_B(11); ISSUE_A(12); ISSUE_B(12); ISSUE_A(13); ISSUE_B(13); SBAR();
  QMM(wb, 15, 0); QMM(wb, 15, 1);
  STAMP(2);
  {
    const int tile = rb * 4 + rg;
    const float qsc = cb < 8 ? QSCALE : 1.0f;
    const u32 qkb = (cb < 8 ? WS_Q + (u32)(((cb * 4 + 2 * ch) * 64 + tile) * 1024) : WS_KV + (u32)(((cb - 8) * 4 + 2 * ch) * 131072 + tile * 2048)) + lane * 16;
    const u32 hstride = cb < 8 ? 65536u : 131072u;
    ST16(pair16(aq[0][0] * qsc, aq[0][1] * qsc), qkb);
    ST16(pair16(aq[1][0] * qsc, aq[1][1] * qsc), qkb + hstride);
    ST16(pair32(aq[2][0], aq[2][1]), WS_KV + (cb * 2 + ch) * 131072 + tile * 2048 + 1024 + lane * 16);
    ST16((u32x4{~0u, ~0u, ~0u, ~0u}), WS_O + ((qb * 8 + w) * 32 + head) * 1024 + lane * 16);
  }
  STAMP(3);
  FBODY(8, , , );
  FSTAGE(9, 12, , , );
  FSTAGE(10, 0, , , );
  if (tid == 0) __hip_atomic_store((GAS u64*)(ws + WS_FLAG1 + b * 8), tag, RLX_AGENT);
  STAMP(4);
  FSTAGE(11, 63, ISSUE_A(14), ISSUE_B(14), );
  const int grow = rb * 128 + cb * 8 + w;
  u32x4 gx = *(const u32x4*)(x + grow * DM + lane * 8), gv = *(const u32x4*)(gw + lane * 8);
  u32x2 b1v[4];
#pragma unroll
  for (int c = 0; c < 4; ++c) b1v[c] = *(const u32x2*)(b1 + cb * 112 + (c == 3 ? 48 : ch * 64 + c * 16) + g4 * 4);
  const int pu = b + 256 * w;
  const bool pdo = pu < 1152, pw2 = pu < 896;
  const int prow = (pw2 ? pu : pu - 896) >> 3, pcg = pu & 7;
#define PTLOAD() do { if (pdo) { \
      const rsrc_t rp = __builtin_amdgcn_make_buffer_rsrc((void*)(pw2 ? w2 : wo), 0, (pw2 ? FF : DM) * DM * 2, 0x00020000); \
      const u32 vo = (u32)((lane >> 3) * (DM * 2) + (lane & 7) * 16), so = (u32)(prow * 16 * (DM * 2) + pcg * 128); \
      dma16(rp, vo, so, lds0 + L_PT + w * 2048); dma16(rp, vo, so + 8 * DM * 2, lds0 + L_PT + w * 2048 + 1024); } } while (0)
#define PTSTORE() do { if (pdo) { \
      const lds_ptr p = lds + L_PT + w * 2048 + ((pw2 ? 8 : 4) * hh + (r16 >> 2)) * 128 + (g4 & 1) * 32 + (r16 & 3) * 8; const int d = pw2 ? 512 : 1024; \
      const u32 o = (pw2 ? WS_W2F + (u32)((pcg * 2 * 112 + prow) * 1024) : WS_WOF + (u32)((pcg * 2 * 32 + prow) * 1024)) + lane * 16; \
      ST16(__builtin_bit_cast(u32x4, trfrag(p, p + d)), o); ST16(__builtin_bit_cast(u32x4, trfrag(p + 64, p + 64 + d)), o + (pw2 ? 112 : 32) * 1024); } } while (0)
  const int prb = lane < 32 ? r16 : 2 * qb + (lane & 1), pcb = lane < 16 ? 8 + (head >> 2) : lane < 32 ? (head >> 1) : (head >> 2);
  const u32 f1off = WS_FLAG1 + (((pcb >> 2) << 1 | (prb >> 3)) | (((pcb & 3) << 3 | (prb & 7)) << 3)) * 8;
  const int qg = w & 3, kh = w >> 2;
  u64 f1v;
  u32x4 qAu, qBu;
#define F1LOAD() asm volatile("global_load_dwordx2 %0, %1, %2 sc1" : "=v"(f1v) : "v"(f1off), "s"(ws) : "memory")
#define F1CHECK() do { \
    asm volatile("s_waitcnt vmcnt(0)" : "+v"(f1v) :: "memory"); \
    asm volatile("" : "+v"(gx), "+v"(gv), "+v"(b1v[0]), "+v"(b1v[1]), "+v"(b1v[2]), "+v"(b1v[3])); \
    for (u32 spins = 0; !__all(f1v == tag);) { \
      if (++spins > SPIN_CAP) { alive = false; break; } \
      __builtin_amdgcn_s_sleep(1); \
      f1v = __hip_atomic_load((GAS u64*)(ws + f1off), RLX_AGENT); } \
    asm volatile("" ::: "memory"); STAMP(6); \
    const u32 qo = WS_Q + (head * 64 + qb * 8 + qg * 2) * 1024 + lane * 16; \
    asm volatile("global_load_dwordx4 %0, %2, %3 sc1\n\tglobal_load_dwordx4 %1, %2, %3 offset:1024 sc1" : "=&v"(qAu), "=&v"(qBu) : "v"(qo), "s"(ws) : "memory"); \
    KVDMA(0); KVDMA(1); } while (0)
  PTLOAD();
  FSTAGE(12, 63, ISSUE_A(15), ISSUE_B(15), F1LOAD());
  FSTAGE(13, 63, , , F1CHECK());
  FSTAGE(14, 63, KVDMA(2); KVDMA(3), KVDMA(4); KVDMA(5), );
  FSTAGE(15, 63, KVDMA(6), KVDMA(7), PTSTORE());
  FMM(wb, 15, 0); FMM(wb, 15, 1);
  STAMP(5);
  BARRIER();
  if ((tid & 7) < 6) { const u32 one = ((tid & 3) == 0) ? 0x3F803F80u : 0u; lds_st16(lds + (tid >> 3) * KVS + 2048 + (tid & 7) * 16, u32x4{one, one, one, one}); }
  {
    const u32 hb = WS_H + (u32)(((rb * 4 + rg) * 112 + cb * 7 + ch * 4) * 1024) + lane * 16;
#pragma unroll
    for (int c = 0; c < 4; ++c) {
      if (c < 3 || ch == 0) {
        const f32x4 bb = unpk4(b1v[c]);
        ST16(pair16(gelu4(ah[c][0] + bb), gelu4(ah[c][1] + bb)), hb + c * 1024);
      }
    }
    float s = 0.f;
#pragma unroll
    for (int e = 0; e < 4; ++e) { const u32 ua = gx[e], ub = gv[e]; s = __builtin_amdgcn_fdot2_f32_bf16(__builtin_bit_cast(bf16x2, ua), __builtin_bit_cast(bf16x2, ub), s, false); }
    const float z = rbf(wave_sum(s)), g = rbf(__builtin_amdgcn_rcpf(1.0f + EX(z * -1.4426950408889634f)));
    if (lane == 0) __builtin_amdgcn_raw_buffer_store_b32(__float_as_uint(g), rs, WS_G + grow * 4, 0, AUX_SC1);
  }
  STAMP(7);
  asm volatile("s_waitcnt vmcnt(4) lgkmcnt(0)\n\ts_barrier" : "+v"(qAu), "+v"(qBu) :: "memory");
  const bf16x8 qA = asbf(qAu), qB = asbf(qBu);
  STAMP(8);

  lds_ptr kp = lds + kh * (32 * KVS) + lane * 16;
  const bool grp2 = (r32 >= 16 && r32 < 20) || r32 >= 28;
  const int coff = 2048 + (r32 == 16 ? 0 : r32 == 20 ? 64 : grp2 ? 16 : 80);
  lds_ptr vp0 = lds + kh * (32 * KVS) + (r32 < 16 ? 1024 + hh * 256 + r32 * 16 : coff), vp1 = lds + kh * (32 * KVS) + (r32 < 16 ? 1536 + hh * 256 + r32 * 16 : coff);
  f32x16 sA, sB, oA = splat16(0.f), oB = splat16(0.f), cn16;
  f32x16 fa[2][2];
  fa[0][0] = splat16(0.f); fa[0][1] = splat16(0.f); fa[1][0] = splat16(0.f); fa[1][1] = splat16(0.f);
  u32x4 kf, v0, v1f, p1 = u32x4{0, 0, 0, 0};
  u32x4 sh[2][2], sw[2][2];
  {
    kf = lds_ld16(kp); v0 = lds_ld16(vp0); v1f = lds_ld16(vp1);
    sA = MFMA32(asbf(kf), qA, splat16(0.f));
    sB = MFMA32(asbf(kf), qB, splat16(0.f));
    cn16 = splat16(-xhalf_max(__builtin_fmaxf(max16(sA), max16(sB))));
    PIN(cn16);
    sA = MFMA32(asbf(kf), qA, cn16);
  }
  const u32 hoff = WS_H + (u32)((tb * 2 * 112 + w * 14) * 1024) + lane * 16;
  const u32 woff = WS_W2F + (u32)((nb * 2 * 112 + w * 14) * 1024) + lane * 16;
  int fk = (tb & 7) + 4 * (nb & 3); fk = fk >= 14 ? fk - 14 : fk;
#define UNIT(SC, SN, QN, OC, OP, RD0, RD1, RD2, FMX, LD) do { \
    SN = MFMA32(asbf(kf), QN, cn16); KEEP(SN); RD0; \
    SBAR(); \
    SC[0] = EX(SC[0]); SC[1] = EX(SC[1]); SC[2] = EX(SC[2]); SC[3] = EX(SC[3]); PIN(SC); \
    SBAR(); \
    OP = MFMA32(asbf(v1f), asbf(p1), OP); KEEP(OP); RD1; \
    SBAR(); \
    u32x4 p0; \
    SC[4] = EX(SC[4]); SC[5] = EX(SC[5]); SC[6] = EX(SC[6]); SC[7] = EX(SC[7]); p0[0] = cvtpk(SC[0], SC[1]); p0[1] = cvtpk(SC[2], SC[3]); PIN(SC); PIN(p0); \
    SBAR(); \
    FMX; LD; \
    SBAR(); \
    p0[2] = cvtpk(SC[4], SC[5]); p0[3] = cvtpk(SC[6], SC[7]); PIN(p0); SBAR(); \
    SC[8] = EX(SC[8]); SC[9] = EX(SC[9]); SC[10] = EX(SC[10]); PIN(SC); \
    SBAR(); \
    OC = MFMA32(asbf(v0), asbf(p0), OC); KEEP(OC); RD2; \
    SBAR(); \
    SC[11] = EX(SC[11]); SC[12] = EX(SC[12]); SC[13] = EX(SC[13]); SC[14] = EX(SC[14]); SC[15] = EX(SC[15]); \
    p1 = pack8(SC, 8); PIN(p1); \
    SBAR(); } while (0)
#define UA(t, FMX, LD) UNIT(sA, sB, qB, oA, oB, kf = lds_ld16(kp + ((t) + 1) * KVS), v1f = lds_ld16(vp1 + (t) * KVS), , FMX, LD)
#define UB(t, FMX, LD) UNIT(sB, sA, qA, oB, oA, , , v0 = lds_ld16(vp0 + ((t) + 1) * KVS), FMX, LD)
#define ADV() do { kp += 4 * KVS; vp0 += 4 * KVS; vp1 += 4 * KVS; } while (0)
#define LDH(s, i, so) sh[s][i] = LD16(hoff, (so) + (i) * (112 * 1024))
#define LDW(s, i, so) sw[s][i] = LD16(woff, (so) + (i) * (112 * 1024))
#define FM(s, n, t) fa[n][t] = MFMA32(asbf(sw[s][n]), asbf(sh[s][t]), fa[n][t]); KEEP(fa[n][t])
#define NEXTK() __extension__({ const int o_ = fk * 1024; fk = fk == 13 ? 0 : fk + 1; o_; })
  UA(0, , KVDMA(8); KVDMA(9); KVDMA(10)); UB(0, , KVDMA(11); KVDMA(12); KVDMA(13)); UA(1, , KVDMA(14); KVDMA(15)); UB(1, , );
  WAITB(0);
  if (tid == 0) __hip_atomic_store((GAS u64*)(ws + WS_FLAG2 + b * 8), tag, RLX_AGENT);
  UA(2, , ); UB(2, , ); UA(3, , ); UB(3, , ); ADV();
  u32x4 f0, f1;
  UA(0, , ); UB(0, , f0 = LD16(WS_FLAG2 + lane * 32, 0); f1 = LD16(WS_FLAG2 + lane * 32 + 16, 0)); UA(1, , ); UB(1, , );
  for (u32 spins = 0; alive;) {
    const u32 lo = (u32)tag, hi = (u32)(tag >> 32);
    const bool ok = f0[0] == lo && f0[1] == hi && f0[2] == lo && f0[3] == hi && f1[0] == lo && f1[1] == hi && f1[2] == lo && f1[3] == hi;
    if (__all(ok)) break;
    if (++spins > SPIN_CAP) { alive = false; break; }
    __builtin_amdgcn_s_sleep(1);
    f0 = LD16(WS_FLAG2 + lane * 32, 0); f1 = LD16(WS_FLAG2 + lane * 32 + 16, 0);
  }
  asm volatile("" ::: "memory");
  if (b == 0 && tid == 0 && alive) __hip_atomic_store((GAS u64*)(ws + WS_EPOCH), tag, RLX_AGENT);
  STAMP(9);
  { const int s0 = NEXTK(), s1 = NEXTK();
    LDH(0, 0, s0); LDW(0, 0, s0); LDW(0, 1, s0); LDH(0, 1, s0); LDH(1, 0, s1); LDW(1, 0, s1); LDW(1, 1, s1); LDH(1, 1, s1); }
  const int tb2 = w & 1, kq = w >> 1;
  const float gq = __uint_as_float(__builtin_amdgcn_raw_buffer_load_b32(rs, WS_G + (qb * 256 + qg * 64 + kh * 32 + r32) * 4, 0, AUX_SC1));
  const float gt = __uint_as_float(__builtin_amdgcn_raw_buffer_load_b32(rs, WS_G + (tb * 64 + tb2 * 32 + r32) * 4, 0, AUX_SC1));
  UA(2, , ); UB(2, , ); UA(3, , ); UB(3, , ); ADV();
  for (int it = 2; it < 8; ++it) {
    const int s0 = NEXTK(), s1 = NEXTK();
    UA(0, FM(0, 0, 0), ); UB(0, FM(0, 1, 0), LDH(0, 0, s0)); UA(1, FM(0, 0, 1), LDW(0, 0, s0)); UB(1, FM(0, 1, 1), LDW(0, 1, s0); LDH(0, 1, s0));
    UA(2, FM(1, 0, 0), ); UB(2, FM(1, 1, 0), LDH(1, 0, s1)); UA(3, FM(1, 0, 1), LDW(1, 0, s1)); UB(3, FM(1, 1, 1), LDW(1, 1, s1); LDH(1, 1, s1));
    ADV();
  }
  oB = MFMA32(asbf(v1f), asbf(p1), oB);
  STAMP(10);
  float cA = cn16[0], cB = cA;
  if (__builtin_expect(!__all(oA[8] <= 0x1p30f && oB[8] <= 0x1p30f && oA[8] >= 0x1p-60f && oB[8] >= 0x1p-60f), 0)) {
    const lds_ptr k2 = kp - 32 * KVS, v2 = vp0 - 32 * KVS, v3 = vp1 - 32 * KVS;
#define REDO(Q, O, C) do { float m = -INFINITY; \
      for (int t = 0; t < 32; ++t) m = __builtin_fmaxf(m, max16(MFMA32(asbf(lds_ld16(k2 + t * KVS)), Q, splat16(0.f)))); \
      C = -xhalf_max(m); cn16 = splat16(C); O = splat16(0.f); \
      for (int t = 0; t < 32; ++t) { \
        f32x16 s = MFMA32(asbf(lds_ld16(k2 + t * KVS)), Q, cn16); \
        _Pragma("unroll") for (int r = 0; r < 16; ++r) s[r] = EX(s[r]); \
        O = MFMA32(asbf(lds_ld16(v2 + t * KVS)), asbf(pack8(s, 0)), O); O = MFMA32(asbf(lds_ld16(v3 + t * KVS)), asbf(pack8(s, 8)), O); } } while (0)
    REDO(qA, oA, cA); REDO(qB, oB, cB);
  }
  int lz;
  asm volatile("v_mbcnt_lo_u32_b32 %0, -1, 0\n\tv_mbcnt_hi_u32_b32 %0, -1, %0" : "=v"(lz));
  const int ncol = nb * 64 + kq * 16;
  const u32x2 bia = *(const u32x2*)(b2 + ncol + 4 * (lz >> 5)), bib = *(const u32x2*)(b2 + ncol + 8 + 4 * (lz >> 5));
  {
    const f32x16 os = kh ? oA : oB;
    const float cs = kh ? cA : cB;
    const lds_ptr p = lds + L_CMB + w * 2560 + lz * 16;
    lds_stf(p, f32x4{os[0], os[1], os[2], os[3]}); lds_stf(p + 1024, f32x4{os[4], os[5], os[6], os[7]});
    *(LAS f32x2*)(lds + L_CMB + w * 2560 + 2048 + lz * 8) = f32x2{os[8], cs};
  }
  BARRIER();
  {
    const f32x16 om = kh ? oB : oA;
    const float cm = kh ? cB : cA;
    const lds_ptr p = lds + L_CMB + (w ^ 4) * 2560 + lz * 16;
    const f32x4 a = lds_ldf(p), c = lds_ldf(p + 1024);
    const f32x2 lc = *(LAS f32x2*)(lds + L_CMB + (w ^ 4) * 2560 + 2048 + lz * 8);
    const float cn = __builtin_fminf(cm, lc[1]);
    float e0 = EX(cn - cm), e1 = EX(cn - lc[1]);
    const float sc = gq * __builtin_amdgcn_rcpf(om[8] * e0 + lc[0] * e1);
    e0 *= sc; e1 *= sc;
    u32x4 o = u32x4{cvtpk(om[0] * e0 + a[0] * e1, om[1] * e0 + a[1] * e1), cvtpk(om[2] * e0 + a[2] * e1, om[3] * e0 + a[3] * e1),
                    cvtpk(om[4] * e0 + c[0] * e1, om[5] * e0 + c[1] * e1), cvtpk(om[6] * e0 + c[2] * e1, om[7] * e0 + c[3] * e1)};
#pragma unroll
    for (int e = 0; e < 4; ++e) o[e] = (o[e] == ~0u) ? 0x7FC07FC0u : o[e];
    ST16(o, WS_O + ((qb * 8 + qg * 2 + kh) * 32 + head) * 1024 + lz * 16);
  }
  STAMP(14);
  SBAR();
  u32x4 wf[8][2], of[8];
#define OLOAD() _Pragma("unroll") for (int j = 0; j < 8; ++j) of[j] = LD16(WS_O + ((tb * 2 + tb2) * 32 + kq * 8 + j) * 1024 + lz * 16, 0)
#pragma unroll
  for (int j = 0; j < 8; ++j) {
    wf[j][0] = LD16(WS_WOF + ((nb * 2) * 32 + kq * 8 + j) * 1024 + lz * 16, 0);
    wf[j][1] = LD16(WS_WOF + ((nb * 2 + 1) * 32 + kq * 8 + j) * 1024 + lz * 16, 0);
  }
  FM(0, 0, 0); FM(0, 1, 0); FM(0, 0, 1); FM(0, 1, 1); FM(1, 0, 0); FM(1, 1, 0); FM(1, 0, 1); FM(1, 1, 1);
#define Q4(a, r) f32x4{(a)[r], (a)[(r) + 1], (a)[(r) + 2], (a)[(r) + 3]}
#pragma unroll
  for (int d = 0; d < 8; ++d) {
    const lds_ptr p = lds + (d * 8 + w) * 2048 + lz * 16;
    lds_stf(p, Q4(fa[d >> 2][d & 1], ((d >> 1) & 1) * 8));
    lds_stf(p + 1024, Q4(fa[d >> 2][d & 1], ((d >> 1) & 1) * 8 + 4));
  }
  OLOAD();
  BARRIER();
  f32x4 lo = unpk4(bia), hi = unpk4(bib);
#pragma unroll
  for (int s = 0; s < 8; ++s) {
    const lds_ptr p = lds + (w * 8 + s) * 2048 + lz * 16;
    lo += lds_ldf(p); hi += lds_ldf(p + 1024);
  }
  { const float og = rbf(1.0f - gt); lo *= og; hi *= og; }
  PIN(lo); PIN(hi);
  BARRIER();
  for (u32 spins = 0;;) {
    bool ok = true;
#pragma unroll
    for (int j = 0; j < 8; ++j) ok = ok && of[j][0] != ~0u && of[j][1] != ~0u && of[j][2] != ~0u && of[j][3] != ~0u;
    if (__all(ok) || !alive) break;
    if (++spins > SPIN_CAP) { alive = false; break; }
    asm volatile("" ::: "memory");
    OLOAD();
  }
  STAMP(11);
  f32x16 pa[2] = {splat16(0.f), splat16(0.f)};
#pragma unroll
  for (int j = 0; j < 8; ++j) { pa[0] = MFMA32(asbf(wf[j][0]), asbf(of[j]), pa[0]); pa[1] = MFMA32(asbf(wf[j][1]), asbf(of[j]), pa[1]); }
#pragma unroll
  for (int q = 0; q < 4; ++q) {
    const lds_ptr p = lds + ((q * 2 + tb2) * 4 + kq) * 2048 + lz * 16;
    lds_stf(p, Q4(pa[q >> 1], (q & 1) * 8));
    lds_stf(p + 1024, Q4(pa[q >> 1], (q & 1) * 8 + 4));
  }
  BARRIER();
#pragma unroll
  for (int s = 0; s < 4; ++s) {
    const lds_ptr p = lds + (w * 4 + s) * 2048 + lz * 16;
    lo += lds_ldf(p); hi += lds_ldf(p + 1024);
  }
  *(u32x4*)(out + (tb * 64 + tb2 * 32 + (lz & 31)) * DM + ncol + 8 * (lz >> 5)) = pair32(lo, hi);
  STAMP(12);
}

extern "C" void kernel_launch(void* const* d_in, const int* in_sizes, int n_in, void* d_out, int out_size, void* d_ws, size_t ws_size,
                              hipStream_t stream) {
  const u16 *x = (const u16*)d_in[0], *wqkv = (const u16*)d_in[1], *wo = (const u16*)d_in[2], *w1 = (const u16*)d_in[4],
            *b1 = (const u16*)d_in[5], *w2 = (const u16*)d_in[6], *b2 = (const u16*)d_in[7], *gw = (const u16*)d_in[8];
  fused_kernel<<<256, 512, 0, stream>>>(x, wqkv, wo, w1, b1, w2, b2, gw, (u16*)d_out, (char*)d_ws);
}
```

```cpp
#include <hip/hip_runtime.h>
#include <hip/hip_bf16.h>
#include <stdint.h>

typedef unsigned short u16;
typedef unsigned int u32;
typedef unsigned long long u64;
typedef __attribute__((ext_vector_type(8))) __bf16 bf16x8;
typedef __attribute__((ext_vector_type(2))) __bf16 bf16x2;
typedef __attribute__((ext_vector_type(4))) float f32x4;
typedef __attribute__((ext_vector_type(2))) float f32x2;
typedef __attribute__((ext_vector_type(16))) float f32x16;
typedef __attribute__((ext_vector_type(4))) u32 u32x4;
typedef __attribute__((ext_vector_type(2))) u32 u32x2;
#define LAS __attribute__((address_space(3)))
#define GAS __attribute__((address_space(1)))
typedef LAS char* lds_ptr;

constexpr int NT0 = 24;
constexpr int FPRI = 4;
constexpr int DM = 512, FF = 1792;
constexpr u32 WS_EPOCH = 0, WS_G = 16384, WS_MB1 = 64u << 10, WS_ALL = 256u << 10;
constexpr u32 WS_Q = 1u << 20, WS_KV = 3u << 20, WS_H = 7u << 20, WS_WOF = 16u << 20, WS_O = 17u << 20, WS_END = 19u << 20;
constexpr int L_X = 0, L_R = 32 * 1024, L_B1 = 96 * 1024, L_JUNK = 100 * 1024, L_W = 104 * 1024, WSLOT = 14336, KVS = 2048;
constexpr int L_PT = 128 * 1024, L_FLG = 132 * 1024, L_CMB = 133 * 1024, L_JUNK2 = 159 * 1024, LDS_BYTES = 160 * 1024;
constexpr int AUX_SC1 = 16;
constexpr int TAIL_OFF = 0x17C0;
constexpr int CODE_LINES = 176;
constexpr u32 SPIN_CAP = 1u << 15;
constexpr float QSCALE = 0.25f * 1.4426950408889634f;

#define SBAR() __builtin_amdgcn_sched_barrier(0)
#define PIN(x) asm volatile("" : "+v"(x))
#define KEEP(x) asm volatile("" :: "v"(x))
#define MFMA32(a, b, c) __builtin_amdgcn_mfma_f32_32x32x16_bf16(a, b, c, 0, 0, 0)
#define MFMA16(a, b, c) __builtin_amdgcn_mfma_f32_16x16x32_bf16(a, b, c, 0, 0, 0)
#define BARRIER() asm volatile("s_waitcnt lgkmcnt(0)\n\ts_barrier" ::: "memory")
#define WAITB_(n) asm volatile("s_waitcnt vmcnt(" #n ") lgkmcnt(0)\n\ts_barrier" ::: "memory")
#define WAITB(n) WAITB_(n)
#define RLX_AGENT __ATOMIC_RELAXED, __HIP_MEMORY_SCOPE_AGENT
#define EX(v) __builtin_amdgcn_exp2f(v)
#define LD16(off, soff) __builtin_amdgcn_raw_buffer_load_b128(rs, off, soff, AUX_SC1)
#define ST16(v, off) __builtin_amdgcn_raw_buffer_store_b128(v, rs, off, 0, AUX_SC1)

__device__ __forceinline__ float bf2f(u32 v) { return __uint_as_float(v << 16); }
__device__ __forceinline__ float bfhi(u32 v) { return __uint_as_float(v & 0xffff0000u); }
__device__ __forceinline__ u32 cvtpk(float lo, float hi) { bf16x2 r = {(__bf16)lo, (__bf16)hi}; return __builtin_bit_cast(u32, r); }
__device__ __forceinline__ float rbf(float f) { return bf2f(cvtpk(f, f)); }
__device__ __forceinline__ bf16x8 asbf(u32x4 v) { return __builtin_bit_cast(bf16x8, v); }
typedef __amdgpu_buffer_rsrc_t rsrc_t;
__device__ __forceinline__ void dma16(rsrc_t r, u32 voff, u32 soff, u32 lds_base) {
  asm volatile("s_mov_b32 m0, %3\n\ts_nop 0\n\tbuffer_load_dwordx4 %0, %1, %2 offen lds" :: "v"(voff), "s"(r), "s"(soff), "s"(lds_base) : "memory");
}
__device__ __forceinline__ void dma16c(rsrc_t r, u32 voff, u32 soff, u32 lds_base) {
  asm volatile("s_mov_b32 m0, %3\n\ts_nop 0\n\tbuffer_load_dwordx4 %0, %1, %2 offen sc1 lds" :: "v"(voff), "s"(r), "s"(soff), "s"(lds_base) : "memory");
}
__device__ __forceinline__ void dma16m(rsrc_t r, u32 voff, u32 soff, u32 lds_base, u64 mask) {
  asm volatile("s_mov_b32 m0, %3\n\ts_mov_b64 exec, %4\n\tbuffer_load_dwordx4 %0, %1, %2 offen lds\n\ts_mov_b64 exec, -1"
               :: "v"(voff), "s"(r), "s"(soff), "s"(lds_base), "s"(mask) : "memory");
}
__device__ __forceinline__ bf16x8 trfrag(lds_ptr p0, lds_ptr p1) {
  typedef short v4i16_t __attribute__((ext_vector_type(4)));
  typedef short v8i16_t __attribute__((ext_vector_type(8)));
  v4i16_t a = __builtin_amdgcn_ds_read_tr16_b64_v4i16((LAS v4i16_t*)p0);
  v4i16_t b = __builtin_amdgcn_ds_read_tr16_b64_v4i16((LAS v4i16_t*)p1);
  v8i16_t r = __builtin_shufflevector(a, b, 0, 1, 2, 3, 4, 5, 6, 7);
  return __builtin_bit_cast(bf16x8, r);
}
__device__ __forceinline__ u32x4 lds_ld16(lds_ptr p) { return *(LAS u32x4*)p; }
__device__ __forceinline__ f32x4 lds_ldf(lds_ptr p) { return *(LAS f32x4*)p; }
__device__ __forceinline__ void lds_st16(lds_ptr p, u32x4 v) { *(LAS u32x4*)p = v; }
__device__ __forceinline__ void lds_stf(lds_ptr p, f32x4 v) { *(LAS f32x4*)p = v; }
__device__ __forceinline__ u32x4 pair16(f32x4 a, f32x4 b) {
  u32 a0 = cvtpk(a[0], a[1]), a1 = cvtpk(a[2], a[3]), b0 = cvtpk(b[0], b[1]), b1 = cvtpk(b[2], b[3]);
  auto s0 = __builtin_amdgcn_permlane16_swap(a0, b0, false, false);
  auto s1 = __builtin_amdgcn_permlane16_swap(a1, b1, false, false);
  return u32x4{s0[0], s1[0], s0[1], s1[1]};
}
__device__ __forceinline__ u32x4 pair32(f32x4 a, f32x4 b) {
  u32 a0 = cvtpk(a[0], a[1]), a1 = cvtpk(a[2], a[3]), b0 = cvtpk(b[0], b[1]), b1 = cvtpk(b[2], b[3]);
  auto s0 = __builtin_amdgcn_permlane32_swap(a0, b0, false, false);
  auto s1 = __builtin_amdgcn_permlane32_swap(a1, b1, false, false);
  return u32x4{s0[0], s1[0], s0[1], s1[1]};
}
__device__ __forceinline__ f32x2 gelu2(f32x2 x) {
  constexpr float K0 = -2.0f * 0.7978845608028654f * 1.4426950408889634f, K1 = K0 * 0.044715f;
  const f32x2 z = x * __builtin_elementwise_fma(x * x, f32x2{K1, K1}, f32x2{K0, K0});
  const f32x2 d = f32x2{EX(z[0]), EX(z[1])} + 1.0f;
  return x * f32x2{__builtin_amdgcn_rcpf(d[0]), __builtin_amdgcn_rcpf(d[1])};
}
__device__ __forceinline__ f32x4 gelu4(f32x4 x) {
  const f32x2 a = gelu2(f32x2{x[0], x[1]}), c = gelu2(f32x2{x[2], x[3]});
  return f32x4{a[0], a[1], c[0], c[1]};
}
__device__ __forceinline__ f32x4 unpk4(u32x2 v) { return f32x4{bf2f(v[0]), bfhi(v[0]), bf2f(v[1]), bfhi(v[1])}; }
__device__ __forceinline__ float xhalf_max(float m) {
  auto r = __builtin_amdgcn_permlane32_swap(__float_as_uint(m), __float_as_uint(m), false, false);
  return __builtin_fmaxf(__uint_as_float(r[0]), __uint_as_float(r[1]));
}
__device__ __forceinline__ float max16(const f32x16& s) {
  float a = __builtin_fmaxf(s[0], s[1]), b = __builtin_fmaxf(s[2], s[3]);
#pragma unroll
  for (int r = 4; r < 16; r += 4) { a = __builtin_fmaxf(__builtin_fmaxf(a, s[r]), s[r + 1]); b = __builtin_fmaxf(__builtin_fmaxf(b, s[r + 2]), s[r + 3]); }
  return __builtin_fmaxf(a, b);
}
__device__ __forceinline__ f32x16 splat16(float v) { return f32x16{v, v, v, v, v, v, v, v, v, v, v, v, v, v, v, v}; }
template <int CTRL> __device__ __forceinline__ float dppadd(float s) {
  return s + __uint_as_float(__builtin_amdgcn_update_dpp(0u, __float_as_uint(s), CTRL, 0xf, 0xf, true));
}
__device__ __forceinline__ float half_sum(float s) {
  s = dppadd<0x128>(s); s = dppadd<0x124>(s); s = dppadd<0x122>(s); s = dppadd<0x121>(s);
  auto a = __builtin_amdgcn_permlane16_swap(__float_as_uint(s), __float_as_uint(s), false, false);
  return __uint_as_float(a[0]) + __uint_as_float(a[1]);
}
__device__ __forceinline__ f32x16 pv8(u32x4 v, const f32x16& s, int r, f32x16 o) {
  typedef __attribute__((ext_vector_type(8))) float f32x8;
  typedef __attribute__((ext_vector_type(16))) __bf16 bf16x16;
  const f32x8 p = {s[r], s[r + 1], s[r + 2], s[r + 3], s[r + 4], s[r + 5], s[r + 6], s[r + 7]};
  return __builtin_amdgcn_smfmac_f32_32x32x32_bf16(asbf(v), __builtin_bit_cast(bf16x16, p), o, (int)0xDDDDDDDDu, 0, 0);
}

#define AX(s) "v_exp_f32 v" #s ", v" #s "\n\t"
#define AUNIT(MP, MS, RD, MC, s0, s1, s2, s3, s4, s5, s6, s7, s8, s9, s10, s11, s12, s13, s14, s15) \
  AX(s0) AX(s1) MP AX(s2) AX(s3) AX(s4) MS RD AX(s5) AX(s6) AX(s7) AX(s8) AX(s9) MC AX(s10) AX(s11) AX(s12) AX(s13) AX(s14) AX(s15)
#define AM(d, a, b, c) "v_mfma_f32_32x32x16_bf16 v[" d "], v[" a "], v[" b "], v[" c "]\n\t"
#define AS(d, a, b) "v_smfmac_f32_32x32x32_bf16 v[" d "], v[" a "], v[" b "], v104\n\t"
#define ARD(ko, v1o, v0r, v0o) "ds_read_b128 v[88:91], v112 offset:" ko "\n\ts_mov_b64 exec, %[m]\n\tds_read_b128 v[100:103], v112 offset:" v1o \
  "\n\tds_read_b128 v[" v0r "], v112 offset:" v0o "\n\ts_mov_b64 exec, -1\n\t"
#define AUA(v0c, RD) AUNIT(AS("16:31", "100:103", "56:63"), AM("48:63", "88:91", "84:87", "64:79"), RD, AS("0:15", v0c, "32:39"), \
                           32, 33, 34, 35, 36, 37, 38, 39, 40, 41, 42, 43, 44, 45, 46, 47)
#define AUB(v0c) AUNIT("s_waitcnt lgkmcnt(0)\n\t" AS("0:15", "100:103", "40:47"), AM("32:47", "88:91", "80:83", "64:79"), "", AS("16:31", v0c, "48:55"), \
                       48, 49, 50, 51, 52, 53, 54, 55, 56, 57, 58, 59, 60, 61, 62, 63)
#define ATTN_OUT "+{v[0:15]}"(oA), "+{v[16:31]}"(oB), "+{v[32:47]}"(sA), "+{v[48:63]}"(sB), "+{v[88:91]}"(kf), "+{v[92:95]}"(va), "+{v[96:99]}"(vb), \
        "+{v[100:103]}"(v1f), "+{v112}"(ka), [c] "+s"(cnt_)
#define ATTN_IN "{v[64:79]}"(cn16), "{v[80:83]}"(qAu), "{v[84:87]}"(qBu), "{v104}"(0xDDDDDDDDu), [m] "s"(0x0000FFFF0000FFFFull)
#define ATTN_LOOP(n) do { int cnt_ = (n); \
    asm volatile("s_nop 1\n1:\n\t" AUA("92:95", ARD("2048", "1280", "96:99", "3072")) AUB("92:95") AUA("96:99", ARD("4096", "3328", "92:95", "5120")) AUB("96:99") \
                 "v_add_u32 v112, 0x1000, v112\n\ts_sub_u32 %[c], %[c], 1\n\ts_cbranch_scc0 1b" : ATTN_OUT : ATTN_IN : "scc", "memory"); } while (0)

__global__ void __launch_bounds__(512, 2)
fused_kernel(const u16* __restrict__ x, const u16* __restrict__ wqkv, const u16* __restrict__ wo, const u16* __restrict__ w1,
             const u16* __restrict__ b1, const u16* __restrict__ w2, const u16* __restrict__ b2, const u16* __restrict__ gw,
             u16* __restrict__ out, char* ws) {
  __shared__ __attribute__((aligned(16))) char smem[LDS_BYTES];
  const lds_ptr lds = (lds_ptr)smem;
  const u32 lds0 = (u32)(uintptr_t)lds;
  const int tid = threadIdx.x, lane = tid & 63, w = __builtin_amdgcn_readfirstlane(tid >> 6);
  const int b = blockIdx.x, xc = b & 7, bi = b >> 3;
#define CODEPF(off, junk) do { u64 pc_; asm volatile("s_getpc_b64 %0" : "=s"(pc_)); \
    asm volatile("s_mov_b32 m0, %2\n\ts_nop 0\n\tglobal_load_lds_dword %0, %1" :: "v"(off), "s"(pc_ & ~127ull), "s"(lds0 + (junk)) : "memory"); } while (0)
  CODEPF((u32)((bi * 8 + w < CODE_LINES ? bi * 8 + w : 0) * 128 + (lane & 31) * 4), L_JUNK);
  const int r16 = lane & 15, g4 = lane >> 4, hh = lane >> 5;
  const rsrc_t rs = __builtin_amdgcn_make_buffer_rsrc(ws, 0, WS_END, 0x00020000);
  const int rb = (xc & 1) * 8 + (bi & 7), cb = (xc >> 1) * 4 + (bi >> 3);
  const int head = xc * 4 + (bi >> 3), qb = bi & 7;
  const int tb = xc * 4 + (bi & 3), nb = bi >> 2;
  bool alive = true;

  const int rg = w & 3, ch = w >> 2, rot = (rb + 2 * cb) & 7;
  const u32 vx = (u32)(((lane >> 3) * DM + ((lane & 7) ^ (lane >> 3)) * 8) * 2);
  const int l12 = (lane * 43) >> 9, c12 = (lane - l12 * 12) ^ ((w & 1) << 1), l14 = (lane * 37) >> 9;
  const u32 vq = (u32)((l12 * 1536 + (c12 < 8 ? cb * 64 + c12 * 8 : 1024 - 64 + cb * 32 + c12 * 8)) * 2);
  const u32 v1 = (u32)((l14 * FF + (lane - l14 * 14) * 8) * 2);
  const rsrc_t rx = __builtin_amdgcn_make_buffer_rsrc((void*)x, 0, 2048 * DM * 2, 0x00020000);
  const rsrc_t rq = __builtin_amdgcn_make_buffer_rsrc((void*)wqkv, 0, DM * 1536 * 2, 0x00020000);
  const rsrc_t r1 = __builtin_amdgcn_make_buffer_rsrc((void*)w1, 0, DM * FF * 2, 0x00020000);
  const u32 xs = (u32)((rb * 128 + 16 * w) * (DM * 2)), qs = (u32)((8 * w) * (1536 * 2)), fs = (u32)((((w >> 1) * 16 + (w & 1) * 4) * FF + cb * 112) * 2);
#define WPOS(s) ((s) < 12 ? L_W + ((s) & 3) * WSLOT : (s) < 14 ? L_R + ((s) - 12) * WSLOT : L_W + ((s) - 14) * WSLOT)
#define ISSUE_A(s) do { const int kc = ((s) + rot) & 7; \
    if ((s) < 8) { \
      dma16(rx, vx, xs + kc * 128, lds0 + L_X + ((s) & 3) * 16384 + w * 2048); \
      dma16(rx, vx, xs + kc * 128 + 8 * DM * 2, lds0 + L_X + ((s) & 3) * 16384 + w * 2048 + 1024); \
    } else if ((s) < 16) { \
      dma16m(r1, v1, fs + kc * (64 * FF * 2), lds0 + WPOS(s) + w * (8 * 224), 0x00FFFFFFFFFFFFFFull); \
    } } while (0)
#define ISSUE_B(s) do { const int kc = ((s) + rot) & 7; \
    if ((s) < 8) { \
      dma16m(rq, vq, qs + kc * (64 * 1536 * 2), lds0 + L_W + ((s) & 3) * WSLOT + w * (8 * 192), 0x0000FFFFFFFFFFFFull); \
      dma16m(rq, vq, qs + kc * (64 * 1536 * 2) + 4 * 1536 * 2, lds0 + L_W + ((s) & 3) * WSLOT + w * (8 * 192) + 4 * 192, 0x0000FFFFFFFFFFFFull); \
    } else if ((s) < 16) { \
      dma16m(r1, v1, fs + kc * (64 * FF * 2) + 8 * FF * 2, lds0 + WPOS(s) + w * (8 * 224) + 4 * 224, 0x00FFFFFFFFFFFFFFull); \
    } } while (0)
  if (w >= 4) __builtin_amdgcn_s_setprio(1);
  ISSUE_A(0); ISSUE_B(0);
  ISSUE_A(1); ISSUE_B(1);
  ISSUE_A(2); ISSUE_B(2);
  u32x2 epoch;
  asm volatile("global_load_dwordx2 %0, %1, %2 sc1" : "=v"(epoch) : "v"(WS_EPOCH), "s"(ws) : "memory");
  u32x4 xf[2][16];
  f32x4 aq[3][2];
  bf16x8 wa[4], wb[4];
#pragma unroll
  for (int t = 0; t < 3; ++t) { aq[t][0] = f32x4{}; aq[t][1] = f32x4{}; }
  const lds_ptr xr0 = lds + L_X + (rg * 32 + ch * 16 + r16) * 128 + ((g4 ^ (r16 & 7)) * 16), xr1 = lds + L_X + (rg * 32 + ch * 16 + r16) * 128 + (((4 + g4) ^ (r16 & 7)) * 16);
  const lds_ptr xr2 = xr0 + (ch ? -2048 : 2048), xr3 = xr1 + (ch ? -2048 : 2048);
  const lds_ptr wqb = lds + L_W + (8 * g4 + (r16 >> 2)) * 192 + (r16 & 3) * 8;
  const lds_ptr wq0 = wqb + ((2 * ch) ^ (g4 & 1)) * 32, wq1 = wqb + ((2 * ch + 1) ^ (g4 & 1)) * 32, wq2 = wqb + ((4 + ch) ^ (g4 & 1)) * 32;
#define QRD(F, s, ks) do { const int o = ((s) & 3) * WSLOT + (ks) * (32 * 192); \
    F[0] = trfrag(wq0 + o, wq0 + o + 4 * 192); F[1] = trfrag(wq1 + o, wq1 + o + 4 * 192); F[2] = trfrag(wq2 + o, wq2 + o + 4 * 192); } while (0)
#define QMM(F, kk, rt) do { const bf16x8 xv = asbf(xf[rt][kk]); \
    aq[0][rt] = MFMA16(F[0], xv, aq[0][rt]); aq[1][rt] = MFMA16(F[1], xv, aq[1][rt]); aq[2][rt] = MFMA16(xv, F[2], aq[2][rt]); \
    KEEP(aq[0][rt]); KEEP(aq[1][rt]); KEEP(aq[2][rt]); SBAR(); } while (0)
#define QSTAGE(s, n) do { WAITB(n); \
    xf[0][2 * (s)] = lds_ld16(xr0 + ((s) & 3) * 16384); xf[1][2 * (s)] = lds_ld16(xr2 + ((s) & 3) * 16384); QRD(wa, s, 0); SBAR(); \
    if ((s) > 0) QMM(wb, 2 * (s) - 1, 0); \
    ISSUE_A((s) + 3); SBAR(); \
    if ((s) > 0) QMM(wb, 2 * (s) - 1, 1); \
    xf[0][2 * (s) + 1] = lds_ld16(xr1 + ((s) & 3) * 16384); xf[1][2 * (s) + 1] = lds_ld16(xr3 + ((s) & 3) * 16384); QRD(wb, s, 1); SBAR(); \
    QMM(wa, 2 * (s), 0); \
    ISSUE_B((s) + 3); SBAR(); \
    QMM(wa, 2 * (s), 1); } while (0)
  QSTAGE(0, 9); QSTAGE(1, 9); QSTAGE(2, 9); QSTAGE(3, 8);
  QSTAGE(4, 8); QSTAGE(5, 8);
  PIN(epoch);
  u64 tag = (((u64)(u32)__builtin_amdgcn_readfirstlane(epoch[1]) << 32) | (u64)(u32)__builtin_amdgcn_readfirstlane(epoch[0])) + 1;
  asm volatile("" : "+s"(tag));
  if (w == 7) dma16m(__builtin_amdgcn_make_buffer_rsrc((void*)b1, 0, FF * 2, 0x00020000), lane * 16, cb * 224, lds0 + L_B1, 0x3FFFull);
  QSTAGE(6, 6); QSTAGE(7, 4);
  f32x4 ah[3][2], ah3;
  const lds_ptr w1h = lds + (16 * (g4 >> 1) + 4 * (g4 & 1) + (r16 >> 2)) * 224 + (r16 & 3) * 8 + 96, w1b = w1h - 96 + ch * 128;
  lds_ptr w1hh = w1h + L_W, w1bh = w1b + L_W;
  PIN(w1hh); PIN(w1bh);
#define FRD(F, s, ks) do { const int o = (WPOS(s) >= L_W ? WPOS(s) - L_W : WPOS(s)) + (ks) * (32 * 224); \
    const lds_ptr p = (WPOS(s) >= L_W ? w1bh : w1b) + o, q = (WPOS(s) >= L_W ? w1hh : w1h) + o; \
    F[0] = trfrag(p, p + 8 * 224); F[1] = trfrag(p + 32, p + 32 + 8 * 224); F[2] = trfrag(p + 64, p + 64 + 8 * 224); \
    F[3] = trfrag(q, q + 8 * 224); } while (0)
#define FMM(F, kk, rt) do { const bf16x8 xv = asbf(xf[rt][kk]); \
    ah[0][rt] = MFMA16(F[0], xv, ah[0][rt]); ah[1][rt] = MFMA16(F[1], xv, ah[1][rt]); ah[2][rt] = MFMA16(F[2], xv, ah[2][rt]); \
    KEEP(ah[0][rt]); KEEP(ah[1][rt]); KEEP(ah[2][rt]); \
    if ((rt) == 0) { ah3 = MFMA16(F[3], xv, ah3); KEEP(ah3); } SBAR(); } while (0)
#define KVDMA(sl) do { const u32 so_ = WS_KV + head * 131072 + (((sl) + qb * 8) & 63) * 2048; \
    dma16c(rs, lane * 16, so_, lds0 + (sl) * KVS); dma16c(rs, lane * 16, so_ + 1024, lds0 + (sl) * KVS + 1024); } while (0)
#define FBODY(s, M0, M1, END) do { \
    FRD(wa, s, 0); SBAR(); \
    if ((s) > 8) FMM(wb, 2 * ((s) - 8) - 1, 0); \
    M0; SBAR(); \
    if ((s) > 8) FMM(wb, 2 * ((s) - 8) - 1, 1); \
    FRD(wb, s, 1); SBAR(); \
    FMM(wa, 2 * ((s) - 8), 0); \
    M1; SBAR(); \
    FMM(wa, 2 * ((s) - 8), 1); \
    END; } while (0)
#define FSTAGE(s, n, M0, M1, END) do { WAITB(n); FBODY(s, M0, M1, END); } while (0)
  WAITB(0);
  ISSUE_A(11); ISSUE_B(11); ISSUE_A(12); ISSUE_B(12); ISSUE_A(13); ISSUE_B(13); SBAR();
  QMM(wb, 15, 0); QMM(wb, 15, 1);

  {
    const int tile = rb * 4 + rg;
    const float qsc = cb < 8 ? QSCALE : 1.0f;
    const u32 qkb = (cb < 8 ? WS_Q + (u32)(((cb * 4 + 2 * ch) * 64 + tile) * 1024) : WS_KV + (u32)(((cb - 8) * 4 + 2 * ch) * 131072 + tile * 2048)) + (lane ^ (ch * 16)) * 16;
    const u32 hstride = cb < 8 ? 65536u : 131072u;
    ST16(pair16(aq[0][0] * qsc, aq[0][1] * qsc), qkb);
    ST16(pair16(aq[1][0] * qsc, aq[1][1] * qsc), qkb + hstride);
    ST16(pair16(aq[2][0], aq[2][1]), WS_KV + (cb * 2 + ch) * 131072 + tile * 2048 + 1024 + (lane ^ (ch * 16)) * 16);
  }

#pragma unroll
  for (int c = 0; c < 3; ++c) { ah[c][0] = unpk4(*(LAS u32x2*)(lds + L_B1 + ch * 128 + c * 32 + g4 * 8)); ah[c][1] = ah[c][0]; }
  ah3 = unpk4(*(LAS u32x2*)(lds + L_B1 + 96 + g4 * 8));
  FBODY(8, , , );
  FBODY(9, , , );
  FBODY(10, , , );
  WAITB(0);
  const u32x2 tag2 = u32x2{(u32)tag, (u32)(tag >> 32)};
  if (tid < 6)
    __builtin_amdgcn_raw_buffer_store_b64(tag2, rs, WS_MB1 + (tid < 4 ? ((cb & 7) * 4 + tid) * 4096 + (cb < 8 ? 256 : 0) : (cb * 2 + tid - 4) * 4096 + 128) + rb * 8, 0, AUX_SC1);

  FBODY(11, ISSUE_A(14); ISSUE_B(14), ISSUE_A(15); ISSUE_B(15), );
  FBODY(12, , , );
#define LANE_ID(l) int l; asm volatile("v_mbcnt_lo_u32_b32 %0, -1, 0\n\tv_mbcnt_hi_u32_b32 %0, -1, %0" : "=v"(l))
#define LDSFLAG(i) ((LAS u32*)(lds + L_FLG + (i) * 4))
#define LDS_SET(i) __hip_atomic_store(LDSFLAG(i), 1u, __ATOMIC_RELAXED, __HIP_MEMORY_SCOPE_WORKGROUP)
#define LDS_GET(i) __hip_atomic_load(LDSFLAG(i), __ATOMIC_RELAXED, __HIP_MEMORY_SCOPE_WORKGROUP)
#define LDS_ARRIVE(i) __hip_atomic_fetch_add(LDSFLAG(i), 1u, __ATOMIC_RELAXED, __HIP_MEMORY_SCOPE_WORKGROUP)
#define LDS_WAIT(i, n, zz) do { _Pragma("nounroll") for (u32 sp_ = 0; LDS_GET(i) < (n) && ++sp_ < (SPIN_CAP << 5);) __builtin_amdgcn_s_sleep(zz); \
    asm volatile("" ::: "memory"); } while (0)
  const u32 f1off = WS_MB1 + head * 4096 + (lane < 32 ? lane * 8 : 256 + (2 * qb + (lane & 1)) * 8);
  const int qg = w & 3, kh = w >> 2;
  u64 f1v;
  u32x4 qAu, qBu;
#define KV1(j) (((j) & 1) * NT0 + 2 * w + ((j) >> 1))
#define F1LOAD() asm volatile("global_load_dwordx2 %0, %1, %2 sc1" : "=v"(f1v) : "v"(f1off), "s"(ws) : "memory")
#define F1CHECK() do { \
    asm volatile("s_waitcnt vmcnt(0)" : "+v"(f1v) :: "memory"); \
    _Pragma("nounroll") for (u32 spins = 0; !__all(f1v == tag) && ++spins < SPIN_CAP;) { \
      __builtin_amdgcn_s_sleep(8); \
      asm volatile("global_load_dwordx2 %0, %1, %2 sc1\n\ts_waitcnt vmcnt(0)" : "=v"(f1v) : "v"(f1off), "s"(ws) : "memory"); } \
    KVDMA(KV1(0)); \
    const u32 qo = WS_Q + (head * 64 + qb * 8 + qg * 2) * 1024 + lane * 16; \
    asm volatile("global_load_dwordx4 %0, %2, %3 sc1\n\tglobal_load_dwordx4 %1, %2, %3 offset:1024 sc1" : "=&v"(qAu), "=&v"(qBu) : "v"(qo), "s"(ws) : "memory"); } while (0)
  if (tid < 16) *(LAS u32*)(lds + L_FLG + tid * 4) = 0;
  FBODY(13, , , F1LOAD());
  FSTAGE(14, 1, , , F1CHECK());
  FBODY(15, KVDMA(KV1(1)), KVDMA(KV1(2)), );
  FMM(wb, 15, 0); FMM(wb, 15, 1);
  KVDMA(KV1(3));

  {
    const u32 hb = WS_H + (u32)(((rb * 4 + rg) * 112 + cb * 7) * 1024);
    u32x4 hv[3];
#pragma unroll
    for (int c = 0; c < 3; ++c) { hv[c] = pair16(gelu4(ah[c][0]), gelu4(ah[c][1])); PIN(hv[c]); }
    const f32x4 y = gelu4(ah3);
#pragma unroll
    for (int c = 0; c < 3; ++c) ST16(hv[c], hb + (ch * 4 + c) * 1024 + (lane ^ (ch * 16)) * 16);
    __builtin_amdgcn_raw_buffer_store_b64(u32x2{cvtpk(y[0], y[1]), cvtpk(y[2], y[3])}, rs, hb + 3 * 1024 + ((g4 >> 1) * 32 + ch * 16 + r16) * 16 + (g4 & 1) * 8, 0, AUX_SC1);
  }

  asm volatile("s_waitcnt vmcnt(4) lgkmcnt(0)\n\ts_barrier" : "+v"(qAu), "+v"(qBu) :: "memory");

#define PUBLISH2() do { asm volatile("s_waitcnt vmcnt(0)" ::: "memory"); LANE_ID(l_); u32 n_ = 0; \
    if (l_ == 0) n_ = LDS_ARRIVE(4); \
    if (__builtin_amdgcn_readfirstlane(n_) == 7 && l_ < 8) \
      __builtin_amdgcn_raw_buffer_store_b64(u32x2{(u32)tag, (u32)(tag >> 32)}, rs, WS_ALL + l_ * 4096 + b * 8, 0, AUX_SC1); } while (0)
  u32x4 gx0, gx1, gv0, gv1;
  if (kh == 0) {
    __builtin_amdgcn_s_setprio(2);
  } else {
    __builtin_amdgcn_s_setprio(3);
#pragma unroll
    for (int j = 0; j < 8; ++j) { const int u = j * 4 + qg; KVDMA(u < NT0 - 16 ? 16 + u : 32 + u); }
    asm volatile("global_load_dwordx4 %0, %4, %6\n\tglobal_load_dwordx4 %1, %4, %6 offset:512\n\tglobal_load_dwordx4 %2, %5, %7\n\tglobal_load_dwordx4 %3, %5, %7 offset:512"
                 : "=&v"(gx0), "=&v"(gx1), "=&v"(gv0), "=&v"(gv1) : "v"(hh * (DM * 2) + (lane & 31) * 16), "v"((lane & 31) * 16), "s"(x + (rb * 128 + cb * 8 + qg * 2) * DM), "s"(gw) : "memory");
    if (w == 4) {
      const rsrc_t rp = __builtin_amdgcn_make_buffer_rsrc((void*)wo, 0, DM * DM * 2, 0x00020000);
      const u32 vo = (u32)((lane >> 3) * (DM * 2) + (lane & 7) * 16), so = (u32)(bi * 16 * (DM * 2) + xc * 128);
      dma16(rp, vo, so, lds0 + L_PT); dma16(rp, vo, so + 8 * DM * 2, lds0 + L_PT + 1024);
    }
  }
  u32 ka = lds0 + kh * (NT0 * KVS) + lane * 16;
  const u32 one = (lane & 27) == 16 ? 0x3F803F80u : 0u;
  const u32x4 cst = u32x4{one, one, one, one};
  f32x16 sA, sB, oA = splat16(0.f), oB = splat16(0.f), cn16;
  u32x4 kf = lds_ld16((lds_ptr)(uintptr_t)ka), va = lds_ld16((lds_ptr)(uintptr_t)(ka + 1024)), vb = cst, v1f = cst;
  va = (lane & 16) ? cst : va;
  sA = MFMA32(asbf(kf), asbf(qAu), splat16(0.f));
  sB = MFMA32(asbf(kf), asbf(qBu), splat16(0.f));
  cn16 = splat16(-xhalf_max(__builtin_fmaxf(max16(sA), max16(sB))));
  sA += cn16;
  sB = splat16(0.f);

  ATTN_LOOP(0);
  if (kh == 1) {
    asm volatile("s_waitcnt vmcnt(0)" : "+v"(gx0), "+v"(gx1), "+v"(gv0), "+v"(gv1) :: "memory");
    LANE_ID(lane);
    if (lane == 0) LDS_ARRIVE(5);
    ST16((u32x4{~0u, ~0u, ~0u, ~0u}), WS_O + ((qb * 8 + qg * 2) * 32 + head) * 1024 + lane * 16);
    ST16((u32x4{~0u, ~0u, ~0u, ~0u}), WS_O + ((qb * 8 + qg * 2 + 1) * 32 + head) * 1024 + lane * 16);
    float s = 0.f;
#pragma unroll
    for (int e = 0; e < 4; ++e) {
      const u32 a0 = gx0[e], b0 = gv0[e], a1 = gx1[e], b1_ = gv1[e];
      s = __builtin_amdgcn_fdot2_f32_bf16(__builtin_bit_cast(bf16x2, a0), __builtin_bit_cast(bf16x2, b0), s, false);
      s = __builtin_amdgcn_fdot2_f32_bf16(__builtin_bit_cast(bf16x2, a1), __builtin_bit_cast(bf16x2, b1_), s, false);
    }
    const float z = rbf(half_sum(s)), g = rbf(__builtin_amdgcn_rcpf(1.0f + EX(z * -1.4426950408889634f)));
    if ((lane & 31) == 0) __builtin_amdgcn_raw_buffer_store_b32(__float_as_uint(g), rs, WS_G + (rb * 128 + cb * 8 + qg * 2 + (lane >> 5)) * 4, 0, AUX_SC1);
    if (w == 4) {
      const lds_ptr p = lds + L_PT + (4 * (lane >> 5) + ((lane & 15) >> 2)) * 128 + ((lane >> 4) & 1) * 32 + (lane & 3) * 8;
      const u32 o = WS_WOF + (u32)((xc * 2 * 32 + bi) * 1024) + lane * 16;
      ST16(__builtin_bit_cast(u32x4, trfrag(p, p + 1024)), o); ST16(__builtin_bit_cast(u32x4, trfrag(p + 64, p + 64 + 1024)), o + 32 * 1024);
    }
    asm volatile("s_waitcnt lgkmcnt(0)" ::: "memory");
  }
  ATTN_LOOP(0);
  PUBLISH2();
  if (kh == 1) __builtin_amdgcn_s_setprio(1);

  ATTN_LOOP(4);
  if (kh == 1) { LANE_ID(l_); if (l_ == 0) LDS_ARRIVE(12); }
  LDS_WAIT(5, 4u, 1);

  ATTN_LOOP(NT0 / 2 - 9 + kh * (32 - NT0));

  const rsrc_t r2 = __builtin_amdgcn_make_buffer_rsrc((void*)w2, 0, FF * DM * 2, 0x00020000);
  float gA = 0.f, gB = 0.f;
  u32x4 hs[4][2], cp[6];
#define HR00 "{v[192:195]}"
#define HR01 "{v[196:199]}"
#define HR10 "{v[200:203]}"
#define HR11 "{v[204:207]}"
#define HR20 "{v[208:211]}"
#define HR21 "{v[212:215]}"
#define HR30 "{v[216:219]}"
#define HR31 "{v[220:223]}"
  int fk = 3 * nb + 7 * (tb & 3); fk = fk >= 28 ? fk - 28 : fk;
#define SLOAD(l, d) do { const u32 at = lds0 + w * 2048 + (d) * 8192, ks = (u32)(w * 28 + fk); fk = fk == 27 ? 0 : fk + 1; \
    const u32 vw = (u32)(((l) >> 3) * (DM * 2) + (((l) & 7) ^ (((l) >> 2) & 4)) * 16); \
    asm volatile("buffer_load_dwordx4 %0, %2, %3, %4 offen sc1\n\tbuffer_load_dwordx4 %1, %2, %3, %5 offen sc1" : "=&" HR##d##0(hs[d][0]), "=&" HR##d##1(hs[d][1]) \
                 : "v"((l) * 16), "s"(rs), "s"(WS_H + (tb * 2 * 112 + ks) * 1024), "s"(WS_H + ((tb * 2 + 1) * 112 + ks) * 1024) : "memory"); \
    dma16(r2, vw, ks * (16 * DM * 2) + nb * 128, at); dma16(r2, vw, ks * (16 * DM * 2) + 8 * DM * 2 + nb * 128, at + 1024); } while (0)
  if (kh == 0) {
    LANE_ID(lane);
#pragma nounroll
    for (u32 spins = 0; LDS_GET(6) == 0;) {
      const u32x4 f0 = LD16(lane * 32, WS_ALL + xc * 4096), f1 = LD16(lane * 32 + 16, WS_ALL + xc * 4096);
      const u32 lo = (u32)tag, hi = (u32)(tag >> 32);
      const bool ok = f0[0] == lo && f0[1] == hi && f0[2] == lo && f0[3] == hi && f1[0] == lo && f1[1] == hi && f1[2] == lo && f1[3] == hi;
      if (__all(ok)) {
        LDS_SET(6);
        if (b == 0 && lane == 0) __hip_atomic_store((GAS u64*)(ws + WS_EPOCH), tag, RLX_AGENT);
        break;
      }
      if (++spins > SPIN_CAP) { alive = false; break; }
      __builtin_amdgcn_s_sleep(16);
    }
    asm volatile("" ::: "memory");

    gA = __uint_as_float(__builtin_amdgcn_raw_buffer_load_b32(rs, WS_G + (qb * 256 + qg * 64 + (lane & 31)) * 4, 0, AUX_SC1));
    gB = __uint_as_float(__builtin_amdgcn_raw_buffer_load_b32(rs, WS_G + (qb * 256 + qg * 64 + 32 + (lane & 31)) * 4, 0, AUX_SC1));
    asm volatile("" : "+v"(gA), "+v"(gB));
    if (lane == 0) LDS_ARRIVE(8);
    LDS_WAIT(8, 4u, 0);
    SLOAD(lane, 0); SLOAD(lane, 1); SLOAD(lane, 2); SLOAD(lane, 3);
  } else {
    LANE_ID(lane);
    CODEPF((u32)(TAIL_OFF + (w & 3) * 256 + lane * 4), L_JUNK2);
    LDS_WAIT(qg, 1u, 1);
#pragma unroll
    for (int e = 0; e < 6; ++e) cp[e] = lds_ld16(lds + L_CMB + qg * 6144 + e * 1024 + lane * 16);
    asm volatile("s_waitcnt lgkmcnt(0)" : "+v"(cp[0]), "+v"(cp[1]), "+v"(cp[2]), "+v"(cp[3]), "+v"(cp[4]), "+v"(cp[5]) :: "memory");
  }

  ATTN_LOOP(0);
  asm volatile("s_nop 1\n\t" AS("16:31", "100:103", "56:63") "s_nop 15\n\ts_nop 7"
               : "+{v[0:15]}"(oA), "+{v[16:31]}"(oB) : "{v[48:63]}"(sB), "{v[100:103]}"(v1f), "{v104}"(0xDDDDDDDDu));

  float cA = cn16[0], cB = cA;
  if (__builtin_expect(!__all(oA[8] <= 0x1p60f && oB[8] <= 0x1p60f && oA[8] >= 0x1p-60f && oB[8] >= 0x1p-60f), 0)) {
    asm volatile("s_waitcnt vmcnt(0)" ::: "memory");
    LANE_ID(lr);
    const int ntl = kh ? 64 - NT0 : NT0;
    const u32 k2 = WS_KV + head * 131072 + lr * 16, o2 = (lr & 27) == 16 ? 0x3F803F80u : 0u;
#define KVT(t) (((t) + kh * NT0 + qb * 8) & 63) * 2048
#define VLD(t, o) ((lr & 16) ? u32x4{o2, o2, o2, o2} : LD16(k2 + (o), KVT(t)))
#define REDO(Q, O, C) do { float m = -INFINITY; \
      for (int t = 0; t < ntl; ++t) m = __builtin_fmaxf(m, max16(MFMA32(asbf(LD16(k2, KVT(t))), asbf(Q), splat16(0.f)))); \
      C = -xhalf_max(m); cn16 = splat16(C); O = splat16(0.f); \
      for (int t = 0; t < ntl; ++t) { \
        f32x16 s = MFMA32(asbf(LD16(k2, KVT(t))), asbf(Q), cn16); \
        _Pragma("unroll") for (int r = 0; r < 16; ++r) s[r] = EX(s[r]); \
        O = pv8(VLD(t, 1024), s, 0, O); O = pv8(VLD(t, 1280), s, 8, O); } } while (0)
    REDO(qAu, oA, cA); REDO(qBu, oB, cB);
  }
  LANE_ID(lz);
#define Q4(a, r) f32x4{(a)[r], (a)[(r) + 1], (a)[(r) + 2], (a)[(r) + 3]}
  const int tb2 = w & 1, kq = w >> 1;
#define WOL(f) ((f) < 28 ? NT0 * KVS + (f) * 1024 : L_PT + ((f) - 28) * 1024)
  f32x16 pa[2];
  u32x4 wf[8][2], of[8];
  const u32 ob = WS_O + ((tb * 2 + tb2) * 32 + kq * 8) * 1024;
#define OL(r, s, o) "buffer_load_dwordx4 v[" r "], %[a], %[rs], %[" s "] offen offset:" o " sc1\n\t"
#define OF_ALL(m) m "{v[224:227]}"(of[0]), m "{v[228:231]}"(of[1]), m "{v[232:235]}"(of[2]), m "{v[236:239]}"(of[3]), \
                  m "{v[240:243]}"(of[4]), m "{v[244:247]}"(of[5]), m "{v[248:251]}"(of[6]), m "{v[252:255]}"(of[7])
#define OFIRST() asm volatile(OL("224:227", "b0", "0") OL("228:231", "b0", "1024") OL("232:235", "b0", "2048") OL("236:239", "b0", "3072") \
                              OL("240:243", "b1", "0") OL("244:247", "b1", "1024") OL("248:251", "b1", "2048") OL("252:255", "b1", "3072") \
                              : OF_ALL("=&") : [a] "v"(lz * 16), [rs] "s"(rs), [b0] "s"(ob), [b1] "s"(ob + 4096) : "memory")
  if (kh == 1) {
#pragma unroll
    for (int t = 0; t < 2; ++t) {
      const f32x16& om = t ? oB : oA;
      const float cm = t ? cB : cA;
      const f32x4 a = __builtin_bit_cast(f32x4, cp[3 * t]), c = __builtin_bit_cast(f32x4, cp[3 * t + 1]);
      const f32x4 lc = __builtin_bit_cast(f32x4, cp[3 * t + 2]);
      const float cn = __builtin_fminf(cm, lc[1]);
      float e0 = EX(cn - cm), e1 = EX(cn - lc[1]);
      const float sc = lc[2] * __builtin_amdgcn_rcpf(om[8] * e0 + lc[0] * e1);
      e0 *= sc; e1 *= sc;
      u32x4 o = u32x4{cvtpk(om[0] * e0 + a[0] * e1, om[1] * e0 + a[1] * e1), cvtpk(om[2] * e0 + a[2] * e1, om[3] * e0 + a[3] * e1),
                      cvtpk(om[4] * e0 + c[0] * e1, om[5] * e0 + c[1] * e1), cvtpk(om[6] * e0 + c[2] * e1, om[7] * e0 + c[3] * e1)};
#pragma unroll
      for (int e = 0; e < 4; ++e) o[e] = (o[e] == ~0u) ? 0x7FC07FC0u : o[e];
      ST16(o, WS_O + ((qb * 8 + qg * 2 + t) * 32 + head) * 1024 + lz * 16);
    }

    pa[0] = splat16(0.f); pa[1] = splat16(0.f);
    asm volatile("s_barrier" ::: "memory");
    OFIRST();
#pragma unroll
    for (int j = 0; j < 8; ++j) { wf[j][0] = lds_ld16(lds + WOL((kq - 2) * 8 + j) + lz * 16); wf[j][1] = lds_ld16(lds + WOL(16 + (kq - 2) * 8 + j) + lz * 16); }
  } else {
  {
    const lds_ptr p = lds + L_CMB + qg * 6144 + lz * 16;
    lds_stf(p, Q4(oA, 0)); lds_stf(p + 1024, Q4(oA, 4)); lds_stf(p + 2048, f32x4{oA[8], cA, gA, 0.f});
    lds_stf(p + 3072, Q4(oB, 0)); lds_stf(p + 4096, Q4(oB, 4)); lds_stf(p + 5120, f32x4{oB[8], cB, gB, 0.f});
    asm volatile("s_waitcnt lgkmcnt(0)" ::: "memory");
    LDS_SET(qg);
  }
  f32x16 fa[2][2];
  fa[0][0] = splat16(0.f); fa[0][1] = splat16(0.f); fa[1][0] = splat16(0.f); fa[1][1] = splat16(0.f);
  float gt;
  const int ncol = nb * 64 + (w >> 1) * 32;
  u32x2 bi4[4];
  {
    const int q = (lz & 15) >> 2;
    const lds_ptr wp = lds + w * 2048 + (8 * (lz >> 5) + q) * 128 + ((lz >> 4) & 1) * 32 + (lz & 3) * 8;
    const lds_ptr wp0 = wp + (q >> 1) * 64, wp1 = wp + ((q >> 1) ^ 1) * 64;
#define SSTEP(d, n, NEXT) do { asm volatile("s_waitcnt vmcnt(" #n ")" : "+" HR##d##0(hs[d][0]), "+" HR##d##1(hs[d][1]) :: "memory"); \
      const bf16x8 w0 = trfrag(wp0 + (d) * 8192, wp0 + (d) * 8192 + 512), w1_ = trfrag(wp1 + (d) * 8192, wp1 + (d) * 8192 + 512); \
      fa[0][0] = MFMA32(w0, asbf(hs[d][0]), fa[0][0]); fa[0][1] = MFMA32(w0, asbf(hs[d][1]), fa[0][1]); \
      fa[1][0] = MFMA32(w1_, asbf(hs[d][0]), fa[1][0]); fa[1][1] = MFMA32(w1_, asbf(hs[d][1]), fa[1][1]); \
      KEEP(fa[0][0]); KEEP(fa[1][0]); KEEP(fa[0][1]); KEEP(fa[1][1]); SBAR(); \
      NEXT; SBAR(); } while (0)
#pragma nounroll
    for (int i = 0; i < 6; ++i) {
      if (i == FPRI) __builtin_amdgcn_s_setprio(0);
      SSTEP(0, 12, SLOAD(lz, 0)); SSTEP(1, 12, SLOAD(lz, 1)); SSTEP(2, 12, SLOAD(lz, 2)); SSTEP(3, 12, SLOAD(lz, 3));
    }
    LDS_WAIT(12, 4u, 1);
#define WODMA(j) dma16c(rs, lz * 16, WS_WOF + ((nb * 2 + (w >> 1)) * 32 + 16 + (w & 1) * 8 + (j)) * 1024, lds0 + WOL(w * 8 + (j)))
    SSTEP(0, 12, WODMA(0); WODMA(1)); SSTEP(1, 10, WODMA(2); WODMA(3)); SSTEP(2, 8, WODMA(4); WODMA(5)); SSTEP(3, 6, WODMA(6); WODMA(7));
    if (lz == 0) LDS_ARRIVE(10);
  }
#pragma unroll
  for (int j = 0; j < 8; ++j) { wf[j][0] = LD16(lz * 16, WS_WOF + ((nb * 2) * 32 + kq * 8 + j) * 1024); wf[j][1] = LD16(lz * 16, WS_WOF + ((nb * 2 + 1) * 32 + kq * 8 + j) * 1024); }
  gt = __uint_as_float(__builtin_amdgcn_raw_buffer_load_b32(rs, WS_G + (tb * 64 + tb2 * 32 + (lz & 31)) * 4, 0, AUX_SC1));
#pragma unroll
  for (int e = 0; e < 4; ++e) bi4[e] = *(const u32x2*)(b2 + ncol + 8 * e + 4 * (lz >> 5));
  LDS_WAIT(10, 4u, 1);
  f32x4 own[4];
#pragma unroll
  for (int d = 0; d < 4; ++d) {
    if (d == w) {
#pragma unroll
      for (int e = 0; e < 4; ++e) own[e] = Q4(fa[d >> 1][d & 1], 4 * e);
    } else {
      const lds_ptr p = lds + (d * 3 + (w > d ? w - 1 : w)) * 4096 + lz * 16;
#pragma unroll
      for (int e = 0; e < 4; ++e) lds_stf(p + e * 1024, Q4(fa[d >> 1][d & 1], 4 * e));
    }
  }
  asm volatile("s_waitcnt lgkmcnt(0)" ::: "memory");
  if (lz == 0) LDS_ARRIVE(9);
  LDS_WAIT(9, 4u, 0);
  {
    const float og = rbf(1.0f - gt);
#pragma unroll
    for (int e = 0; e < 4; ++e) {
      f32x4 v = unpk4(bi4[e]) + own[e];
#pragma unroll
      for (int s = 0; s < 3; ++s) v += lds_ldf(lds + (w * 3 + s) * 4096 + e * 1024 + lz * 16);
#pragma unroll
      for (int r = 0; r < 4; ++r) { pa[0][4 * e + r] = kq ? 0.f : v[r] * og; pa[1][4 * e + r] = kq ? v[r] * og : 0.f; }
    }
    PIN(pa[0]); PIN(pa[1]);
  }
  CODEPF((u32)(w * 1024 + lz * 16), L_JUNK2);

  asm volatile("s_waitcnt vmcnt(0)\n\ts_barrier" ::: "memory");
  OFIRST();
  }
#pragma unroll
  for (int j = 0; j < 8; ++j) { PIN(wf[j][0]); PIN(wf[j][1]); }
  asm volatile("s_nop 1" : "+v"(pa[0]), "+v"(pa[1]));
  {
    u32 pend = 0xffu, miss = 0xffu;
    const u32 cap = alive ? SPIN_CAP : 4u;
#define OUSE(W, j, r0, r1, r2, r3) do { u32 t_; \
      asm volatile(W "s_bitcmp0_b32 %4, %8\n\ts_cbranch_scc1 2f\n\t" \
                   "v_max3_u32 %3, v" #r0 ", v" #r1 ", v" #r2 "\n\tv_max_u32 %3, %3, v" #r3 "\n\tv_cmp_eq_u32 vcc, -1, %3\n\ts_cbranch_vccnz 1f\n\ts_bitset0_b32 %4, %8\n2:\n\t" \
                   "s_and_b32 vcc_lo, %2, %9\n\ts_cmp_lg_u32 vcc_lo, %10\n\ts_cbranch_scc1 1f\n\t" \
                   "v_mfma_f32_32x32x16_bf16 %0, %5, %7, %0\n\tv_mfma_f32_32x32x16_bf16 %1, %6, %7, %1\n\ts_bitset0_b32 %2, %8\n1:" \
                   : "+v"(pa[0]), "+v"(pa[1]), "+s"(pend), "=&v"(t_), "+s"(miss) \
                   : "v"(wf[j][0]), "v"(wf[j][1]), "{v[" #r0 ":" #r3 "]}"(of[j]), "i"(j), "i"((2 << j) - 1), "i"(1 << j) : "scc", "vcc"); } while (0)
#define OPASS(W7, W6, W5, W4, W3, W2, W1, W0) do { OUSE(W7, 0, 224, 225, 226, 227); OUSE(W6, 1, 228, 229, 230, 231); OUSE(W5, 2, 232, 233, 234, 235); \
      OUSE(W4, 3, 236, 237, 238, 239); OUSE(W3, 4, 240, 241, 242, 243); OUSE(W2, 5, 244, 245, 246, 247); OUSE(W1, 6, 248, 249, 250, 251); OUSE(W0, 7, 252, 253, 254, 255); \
      pend = __builtin_amdgcn_readfirstlane(pend); miss = __builtin_amdgcn_readfirstlane(miss); } while (0)
#define VW(n) "s_waitcnt vmcnt(" #n ")\n\t"
    OPASS(VW(7), VW(6), VW(5), VW(4), VW(3), VW(2), VW(1), VW(0));
#define OR(j, r, s, o) "s_bitcmp0_b32 %[ms], " #j "\n\ts_cbranch_scc1 9f\n\t" OL(r, s, o) "9:\n\t"
#pragma nounroll
    for (u32 spins = 0; pend && spins <= cap; ++spins) {
      asm volatile(OR(0, "224:227", "b0", "0") OR(1, "228:231", "b0", "1024") OR(2, "232:235", "b0", "2048") OR(3, "236:239", "b0", "3072")
                   OR(4, "240:243", "b1", "0") OR(5, "244:247", "b1", "1024") OR(6, "248:251", "b1", "2048") OR(7, "252:255", "b1", "3072") "s_waitcnt vmcnt(0)"
                   : OF_ALL("+") : [a] "v"(lz * 16), [rs] "s"(rs), [b0] "s"(ob), [b1] "s"(ob + 4096), [ms] "s"(miss) : "scc", "memory");
      OPASS("", "", "", "", "", "", "", "");
    }
    asm volatile("s_nop 15\n\ts_nop 3" : "+v"(pa[0]), "+v"(pa[1]));
  }

  f32x4 lo, hi;
#pragma unroll
  for (int q = 0; q < 4; ++q) {
    const f32x4 a = Q4(pa[q >> 1], (q & 1) * 8), c = Q4(pa[q >> 1], (q & 1) * 8 + 4);
    if (q == kq) { lo = a; hi = c; }
    else { const lds_ptr p = lds + 80 * 1024 + ((q * 2 + tb2) * 3 + (kq > q ? kq - 1 : kq)) * 2048 + lz * 16; lds_stf(p, a); lds_stf(p + 1024, c); }
  }
  BARRIER();
#pragma unroll
  for (int s = 0; s < 3; ++s) { const lds_ptr p = lds + 80 * 1024 + (w * 3 + s) * 2048 + lz * 16; lo += lds_ldf(p); hi += lds_ldf(p + 1024); }
  *(u32x4*)(out + (tb * 64 + tb2 * 32 + (lz & 31)) * DM + nb * 64 + kq * 16 + 8 * (lz >> 5)) = pair32(lo, hi);

}

extern "C" void kernel_launch(void* const* d_in, const int* in_sizes, int n_in, void* d_out, int out_size, void* d_ws, size_t ws_size,
                              hipStream_t stream) {
  const u16 *x = (const u16*)d_in[0], *wqkv = (const u16*)d_in[1], *wo = (const u16*)d_in[2], *w1 = (const u16*)d_in[4],
            *b1 = (const u16*)d_in[5], *w2 = (const u16*)d_in[6], *b2 = (const u16*)d_in[7], *gw = (const u16*)d_in[8];
  fused_kernel<<<256, 512, 0, stream>>>(x, wqkv, wo, w1, b1, w2, b2, gw, (u16*)d_out, (char*)d_ws);
}
```
